```python
import torch

def _softmax(x, axis=-1):
    return torch.softmax(x, dim=axis)

def _gelu(x):
    return torch.nn.functional.gelu(x, approximate='tanh')

def _silu(x):
    return torch.nn.functional.silu(x)

def _sigmoid(x):
    return torch.sigmoid(x)

def _layernorm(x, axis=-1, eps=1e-05):
    m = x.mean(dim=axis, keepdim=True)
    v = x.var(dim=axis, keepdim=True, unbiased=False)
    return (x - m) / torch.sqrt(v + eps)

def _scatter_add(tgt, idx, src, axis=0):
    return tgt.clone().index_add_(axis, idx, src)

def _segment_sum(src, seg, n):
    out = torch.zeros((n,) + src.shape[1:], dtype=src.dtype)
    return out.index_add_(0, seg, src)

def _pad(x, pw):
    flat = tuple((v for pair in reversed(pw) for v in pair))
    return torch.nn.functional.pad(x, flat)
kernel_name = 'recurrent_scan__memory_aug_recurrent_mem_tr__dense_activation_squared_relu__prompt'

def setup_inputs():

    _w = lambda a: (lambda t: t.to(torch.bfloat16) if t.is_floating_point() else t)(torch.as_tensor(a))

    def _A_setup():
        B, S, Dm = (1, 2048, 640)
        H, Dh = (32, 128)
        M, seg = (8, 32)
        seg = min(seg, S)
        S = S // seg * seg
        return {'x': _w((lambda *s: torch.randn(*s, dtype=torch.bfloat16) * 0.05)(B, S, Dm)), 'mem0': _w((lambda *s: torch.randn(*s, dtype=torch.bfloat16) * 0.05)(B, M, Dm)), 'Wq': _w((lambda *s: torch.randn(*s, dtype=torch.bfloat16) * 1.6)(Dm, H, Dh) * Dm ** (-0.5)), 'Wk': _w((lambda *s: torch.randn(*s, dtype=torch.bfloat16) * 1.6)(Dm, H, Dh) * Dm ** (-0.5)), 'Wv': _w((lambda *s: torch.randn(*s, dtype=torch.bfloat16) * 0.05)(Dm, H, Dh) * Dm ** (-0.5)), 'Wo': _w((lambda *s: torch.randn(*s, dtype=torch.bfloat16) * 0.05)(H, Dh, Dm) * (H * Dh) ** (-0.5)), 'seg_len': seg}
    _A_in = _A_setup()

    def _B_setup():
        B, S, D, F = (1, 2048, 640, 1728)
        return {'x': _w((lambda *s: torch.randn(*s, dtype=torch.bfloat16) * 0.05)(B, S, D)), 'w1': _w((lambda *s: torch.randn(*s, dtype=torch.bfloat16) * 0.05)(D, F)), 'b1': _w((lambda *s: torch.randn(*s, dtype=torch.bfloat16) * 0.4)(F)), 'w2': _w((lambda *s: torch.randn(*s, dtype=torch.bfloat16) * 0.05)(F, D)), 'b2': _w((lambda *s: torch.randn(*s, dtype=torch.bfloat16) * 3.2)(D))}
    _B_in = _B_setup()
    return {'A_x': _A_in['x'], 'A_mem0': _A_in['mem0'], 'A_Wq': _A_in['Wq'], 'A_Wk': _A_in['Wk'], 'A_Wv': _A_in['Wv'], 'A_Wo': _A_in['Wo'], 'A_seg_len': _A_in['seg_len'], 'B_x': _B_in['x'], 'B_w1': _B_in['w1'], 'B_b1': _B_in['b1'], 'B_w2': _B_in['w2'], 'B_b2': _B_in['b2']}

def reference(A_x, A_mem0, A_Wq, A_Wk, A_Wv, A_Wo, A_seg_len, B_x, B_w1, B_b1, B_w2, B_b2):

    def _A_ref(x, mem0, Wq, Wk, Wv, Wo, seg_len):
        B, S, Dm = x.shape
        H, Dh = (Wq.shape[1], Wq.shape[2])
        M = mem0.shape[1]
        scale = Dh ** (-0.5)
        n_seg = S // seg_len
        L = M + seg_len
        idx = torch.arange(L)
        mask = (idx[:, None] < M) | (idx[:, None] >= idx[None, :])
        mem = mem0
        outs = []
        for s in range(n_seg):
            xs = x[:, s * seg_len:(s + 1) * seg_len, :].clone()
            h = torch.concatenate([mem, xs], axis=1)
            q = torch.einsum('btd,dhe->bhte', h, Wq)
            k = torch.einsum('btd,dhe->bhte', h, Wk)
            v = torch.einsum('btd,dhe->bhte', h, Wv)
            scores = torch.einsum('bhqe,bhke->bhqk', q, k) * scale
            scores = torch.where(mask[None, None], scores, -1000000000.0)
            p = _softmax(scores, axis=-1)
            o = torch.einsum('bhqk,bhke->bhqe', p, v)
            o = torch.einsum('bhte,hed->btd', o, Wo)
            mem = o[:, :M, :].clone()
            outs.append(o[:, M:, :])
        return torch.concatenate(outs, axis=1)

    def _B_ref(x, w1, b1, w2, b2):
        h = torch.einsum('bsd,df->bsf', x, w1) + b1
        zero = torch.zeros_like(h)
        r = torch.where(h > zero, h, zero)
        h = r * r
        out = torch.einsum('bsf,fd->bsd', h, w2) + b2
        return out
    x = A_x
    xn = _layernorm(x, axis=-1)
    s = _A_ref(xn, A_mem0, A_Wq, A_Wk, A_Wv, A_Wo, A_seg_len)
    g = _silu(_B_ref(xn, B_w1, B_b1, B_w2, B_b2))
    return x + s * g
```

```cpp
#include <hip/hip_runtime.h>
#include <stdint.h>

typedef unsigned v4u __attribute__((ext_vector_type(4)));
typedef unsigned v2u __attribute__((ext_vector_type(2)));
typedef float f4 __attribute__((ext_vector_type(4)));
typedef float f2 __attribute__((ext_vector_type(2)));
typedef __bf16 bf2 __attribute__((ext_vector_type(2)));
typedef __bf16 bf8 __attribute__((ext_vector_type(8)));
typedef __bf16 bf16v __attribute__((ext_vector_type(16)));
typedef unsigned v8u __attribute__((ext_vector_type(8)));
typedef short s4 __attribute__((ext_vector_type(4)));
#define LAS __attribute__((address_space(3)))
#define GAS __attribute__((address_space(1)))
#define DEVI __device__ __forceinline__

constexpr int NSEG = 64;
constexpr unsigned SENT = 0xFFFFFFFFu;
constexpr int OOB = (int)0x80000000u;
constexpr int MB = 1 << 20;
constexpr int W_CTL = 0;
constexpr int W_MEM = 1 * MB;
constexpr int W_P   = 3 * MB;
constexpr int W_XN  = 6 * MB;
constexpr int W_G   = 9 * MB;
constexpr int W_KM  = 12 * MB;
constexpr int W_PT  = 16 * MB;
constexpr int W_H   = 22 * MB;
constexpr int W_KX  = 30 * MB;
constexpr int W_U   = 48 * MB;
constexpr int W_Z   = 70 * MB;
constexpr int W_DBG = 160 * MB;
constexpr int WS_BYTES = 200 * MB;
constexpr int C_KP = 0;
constexpr int C_ZP = 256;
constexpr int C_QP = 512;
constexpr int C_HP = 768;
constexpr int C_GP = 1024;
constexpr int C_TASK = 1280;
constexpr int C_ERR = 1536;
constexpr unsigned SPIN_CAP = 1u << 18;

struct Args {
  const uint16_t* x; const uint16_t* mem0; const uint16_t* wq; const uint16_t* wk; const uint16_t* wv; const uint16_t* wo;
  const uint16_t* w1; const uint16_t* b1; const uint16_t* w2; const uint16_t* b2; uint16_t* out; unsigned char* ws;
};

typedef __amdgpu_buffer_rsrc_t rsrc_t;
DEVI rsrc_t mkrs(const void* p, int bytes) { return __builtin_amdgcn_make_buffer_rsrc((void*)p, 0, bytes, 0x00020000); }
DEVI v4u ld16c(rsrc_t r, int off) { return __builtin_amdgcn_raw_buffer_load_b128(r, off, 0, 16); }
DEVI v4u ld16(rsrc_t r, int off) { return __builtin_amdgcn_raw_buffer_load_b128(r, off, 0, 0); }
DEVI v2u ld8c(rsrc_t r, int off) { return __builtin_amdgcn_raw_buffer_load_b64(r, off, 0, 16); }
DEVI v2u ld8(rsrc_t r, int off) { return __builtin_amdgcn_raw_buffer_load_b64(r, off, 0, 0); }
DEVI unsigned ld4c(rsrc_t r, int off) { return __builtin_amdgcn_raw_buffer_load_b32(r, off, 0, 16); }
DEVI void st16c(rsrc_t r, int off, v4u v) { __builtin_amdgcn_raw_buffer_store_b128(v, r, off, 0, 16); }
DEVI void st8c(rsrc_t r, int off, v2u v) { __builtin_amdgcn_raw_buffer_store_b64(v, r, off, 0, 16); }
DEVI void st4c(rsrc_t r, int off, unsigned v) { __builtin_amdgcn_raw_buffer_store_b32(v, r, off, 0, 16); }
DEVI void st8(rsrc_t r, int off, v2u v) { __builtin_amdgcn_raw_buffer_store_b64(v, r, off, 0, 0); }
DEVI void st16(rsrc_t r, int off, v4u v) { __builtin_amdgcn_raw_buffer_store_b128(v, r, off, 0, 0); }

DEVI unsigned pk(float lo, float hi) { f2 v = {lo, hi}; return __builtin_bit_cast(unsigned, __builtin_convertvector(v, bf2)); }
DEVI float blo(unsigned u) { return __builtin_bit_cast(float, u << 16); }
DEVI float bhi(unsigned u) { return __builtin_bit_cast(float, u & 0xFFFF0000u); }
DEVI float rbf(float x) { return blo(pk(x, x)); }
DEVI unsigned nosent(unsigned u) { return u < 0xFFFFFFFEu ? u : 0xFFFFFFFEu; }
DEVI bool valid4(v4u d) { return (d.x != SENT) & (d.y != SENT) & (d.z != SENT) & (d.w != SENT); }
DEVI bf8 asbf8(v4u v) { return __builtin_bit_cast(bf8, v); }
DEVI bf16v cat16(bf8 a, bf8 b) { return __builtin_shufflevector(a, b, 0, 1, 2, 3, 4, 5, 6, 7, 8, 9, 10, 11, 12, 13, 14, 15); }
DEVI f4 mfma(bf8 a, bf8 b, f4 c) { return __builtin_amdgcn_mfma_f32_16x16x32_bf16(a, b, c, 0, 0, 0); }
DEVI f4 smfma(bf8 a, bf8 b0, bf8 b1, f4 c, int idx) { return __builtin_amdgcn_smfmac_f32_16x16x64_bf16(a, cat16(b0, b1), c, idx, 0, 0); }
DEVI void bar() { asm volatile("s_waitcnt lgkmcnt(0)\n\ts_barrier" ::: "memory"); }
DEVI void drain() { asm volatile("s_waitcnt vmcnt(0)" ::: "memory"); }
DEVI v4u lds16(const unsigned char* l, int off) { return *(const LAS v4u*)(LAS const unsigned char*)(l + off); }
DEVI f4 ldsf4(const unsigned char* l, int off) { return *(const LAS f4*)(LAS const unsigned char*)(l + off); }
DEVI void sts16(unsigned char* l, int off, v4u v) { *(LAS v4u*)(LAS unsigned char*)(l + off) = v; }
DEVI void stsf4(unsigned char* l, int off, f4 v) { *(LAS f4*)(LAS unsigned char*)(l + off) = v; }
DEVI void sts8(unsigned char* l, int off, v2u v) { *(LAS v2u*)(LAS unsigned char*)(l + off) = v; }
DEVI void sts4(unsigned char* l, int off, unsigned v) { *(LAS unsigned*)(LAS unsigned char*)(l + off) = v; }
DEVI v2u ldtr(const unsigned char* l, int off) {
  return __builtin_bit_cast(v2u, __builtin_amdgcn_ds_read_tr16_b64_v4i16((LAS s4*)(LAS unsigned char*)(l + off)));
}
DEVI bf8 ldtr2(const unsigned char* l, int off0, int off1) { v2u a = ldtr(l, off0), b = ldtr(l, off1); v4u v = {a.x, a.y, b.x, b.y}; return asbf8(v); }
DEVI float u2f(unsigned u) { return __builtin_bit_cast(float, u); }
DEVI float swapadd32(float a, float b) {
  auto r = __builtin_amdgcn_permlane32_swap(__builtin_bit_cast(unsigned, a), __builtin_bit_cast(unsigned, b), false, false);
  return u2f(r[0]) + u2f(r[1]);
}
template <int CTRL> DEVI float dpp(float x) { return __builtin_bit_cast(float, __builtin_amdgcn_mov_dpp(__builtin_bit_cast(int, x), CTRL, 0xf, 0xf, true)); }
template <int CTRL> DEVI unsigned dppu(unsigned x) { return (unsigned)__builtin_amdgcn_mov_dpp((int)x, CTRL, 0xf, 0xf, true); }
DEVI float row16_max(float x) { x = fmaxf(x, dpp<0x128>(x)); x = fmaxf(x, dpp<0x124>(x)); x = fmaxf(x, dpp<0x122>(x)); x = fmaxf(x, dpp<0x121>(x)); return x; }
DEVI float row16_sum(float x) { x += dpp<0x128>(x); x += dpp<0x124>(x); x += dpp<0x122>(x); x += dpp<0x121>(x); return x; }
DEVI float x4_max(float x) {
  auto s = __builtin_amdgcn_permlane16_swap(__builtin_bit_cast(unsigned, x), __builtin_bit_cast(unsigned, x), false, false);
  x = fmaxf(u2f(s[0]), u2f(s[1]));
  auto t = __builtin_amdgcn_permlane32_swap(__builtin_bit_cast(unsigned, x), __builtin_bit_cast(unsigned, x), false, false);
  return fmaxf(u2f(t[0]), u2f(t[1]));
}
DEVI float x4_sum(float x) {
  auto s = __builtin_amdgcn_permlane16_swap(__builtin_bit_cast(unsigned, x), __builtin_bit_cast(unsigned, x), false, false);
  x = u2f(s[0]) + u2f(s[1]);
  auto t = __builtin_amdgcn_permlane32_swap(__builtin_bit_cast(unsigned, x), __builtin_bit_cast(unsigned, x), false, false);
  return u2f(t[0]) + u2f(t[1]);
}

#define STAMP(slot) do {} while (0)

DEVI bool give_up(unsigned& spins, rsrc_t ws) {
  ++spins;
  if (spins > SPIN_CAP) { st4c(ws, W_CTL + C_ERR, 1u); return true; }
  if ((spins & 255u) == 0u) { unsigned e = ld4c(ws, W_CTL + C_ERR); if (__builtin_amdgcn_readfirstlane(e) != 0u) return true; }
  return false;
}
DEVI unsigned wait_prog(rsrc_t ws, int off, int n, unsigned need, int lane, int slp) {
  unsigned spins = 0, mn;
  const int o = lane < n ? W_CTL + off + lane * 4 : OOB;
  for (;;) {
    asm volatile("" ::: "memory");
    unsigned v = ld4c(ws, o);
    v = lane < n ? v : 0xFFFFu;
    bool ok = v >= need;
    if (__all(ok)) {
      for (int m = 32; m >= 1; m >>= 1) v = min(v, (unsigned)__shfl_xor((int)v, m));
      mn = __builtin_amdgcn_readfirstlane(v);
      break;
    }
    if (give_up(spins, ws)) { mn = 0xFFFFu; break; }
    for (int i = 0; i < slp; ++i) __builtin_amdgcn_s_sleep(2);
  }
  return mn;
}

DEVI void load_w256(bf8 (&w)[2][20], rsrc_t rL, int offL, rsrc_t rR, int offR, int ldb, int validcols, unsigned char* lds, int tid) {
  const int lane = tid & 63, wave = __builtin_amdgcn_readfirstlane(tid >> 6);
  constexpr int RS = 576, CH = 128 * RS;
  const int uc = tid & 31, r0 = tid >> 5;
  v4u st[40];
  const bool okc = uc * 8 < validcols;
  if (uc < 16) {
  }
#pragma unroll
  for (int j = 0; j < 40; ++j) {
    const int row = r0 + 16 * j;
    const int oL = (uc < 16 && okc) ? offL + row * ldb + uc * 16 : OOB;
    const int oR = (uc >= 16 && okc) ? offR + row * ldb + (uc - 16) * 16 : OOB;
    v4u a = ld16(rL, oL), b = ld16(rR, oR);
    st[j] = a | b;
  }
  const int g = lane >> 4, a4 = (lane & 15) >> 2, c = lane & 3;
  const int rb = (8 * g + a4) * RS + wave * 64 + c * 16;
#pragma unroll
  for (int ch = 0; ch < 5; ++ch) {
    const int base = (ch & 1) * CH;
#pragma unroll
    for (int j = 0; j < 8; ++j) sts16(lds, base + (r0 + 16 * j) * RS + uc * 16, st[ch * 8 + j]);
    bar();
#pragma unroll
    for (int k = 0; k < 4; ++k)
#pragma unroll
      for (int u = 0; u < 2; ++u) {
        const int o = base + rb + k * 32 * RS + u * 8;
        w[u][ch * 4 + k] = ldtr2(lds, o, o + 4 * RS);
      }
  }
  bar();
}
DEVI void load_w256s(bf8 (&w)[2][20], rsrc_t r, int off, int ldb, int validcols, unsigned char* lds, int tid) {
  const int lane = tid & 63, wave = __builtin_amdgcn_readfirstlane(tid >> 6);
  constexpr int RS = 576, CH = 128 * RS;
  const int uc = tid & 31, r0 = tid >> 5;
  v4u st[40];
  const int o0 = (uc * 8 < validcols) ? off + r0 * ldb + uc * 16 : OOB;
  const int step = (uc * 8 < validcols) ? 16 * ldb : 0;
#pragma unroll
  for (int j = 0; j < 40; ++j) st[j] = ld16(r, o0 + j * step);
  const int g = lane >> 4, a4 = (lane & 15) >> 2, c = lane & 3;
  const int rb = (8 * g + a4) * RS + wave * 64 + c * 16;
#pragma unroll
  for (int ch = 0; ch < 5; ++ch) {
    const int base = (ch & 1) * CH;
#pragma unroll
    for (int j = 0; j < 8; ++j) sts16(lds, base + (r0 + 16 * j) * RS + uc * 16, st[ch * 8 + j]);
    bar();
#pragma unroll
    for (int k = 0; k < 4; ++k)
#pragma unroll
      for (int u = 0; u < 2; ++u) {
        const int o = base + rb + k * 32 * RS + u * 8;
        w[u][ch * 4 + k] = ldtr2(lds, o, o + 4 * RS);
      }
  }
  bar();
}
DEVI void load_wvo(bf8 (&wv)[20], bf8 (&wo)[5][4], const Args& a, int h, unsigned char* lds, int tid) {
  const int lane = tid & 63, wave = __builtin_amdgcn_readfirstlane(tid >> 6);
  const rsrc_t rv = mkrs(a.wv, 640 * 4096 * 2), ro = mkrs(a.wo, 640 * 4096 * 2);
  v4u st[40];
  {
    const int uc = tid & 15, r0 = tid >> 4;
#pragma unroll
    for (int j = 0; j < 20; ++j) st[j] = ld16(rv, (r0 + 32 * j) * 8192 + h * 256 + uc * 16);
#pragma unroll
    for (int j = 0; j < 20; ++j) st[20 + j] = ld16(ro, h * 163840 + (j * 512 + tid) * 16);
  }
  const int g = lane >> 4, a4 = (lane & 15) >> 2, c = lane & 3;
  {
    constexpr int RS = 320, CH = 160 * RS;
    const int uc = tid & 15, r0 = tid >> 4;
    const int rb = (8 * g + a4) * RS + (wave >> 1) * 64 + c * 16 + (wave & 1) * 8;
#pragma unroll
    for (int ch = 0; ch < 4; ++ch) {
      const int base = (ch & 1) * CH;
#pragma unroll
      for (int j = 0; j < 5; ++j) sts16(lds, base + (r0 + 32 * j) * RS + uc * 16, st[ch * 5 + j]);
      bar();
#pragma unroll
      for (int k = 0; k < 5; ++k) { const int o = base + rb + k * 32 * RS; wv[ch * 5 + k] = ldtr2(lds, o, o + 4 * RS); }
    }
    bar();
  }
  {
    constexpr int RS = 1280 + 64, CH = 32 * RS;
    const int rb = (8 * g + a4) * RS + wave * 160 + c * 8;
#pragma unroll
    for (int ch = 0; ch < 4; ++ch) {
      const int base = (ch & 1) * CH;
#pragma unroll
      for (int j = 0; j < 5; ++j) { const int u = j * 512 + tid; const int row = u / 80, uc = u - row * 80; sts16(lds, base + row * RS + uc * 16, st[20 + ch * 5 + j]); }
      bar();
#pragma unroll
      for (int t = 0; t < 5; ++t) { const int o = base + rb + t * 32; wo[t][ch] = ldtr2(lds, o, o + 4 * RS); }
    }
    bar();
  }
}

constexpr int L_M = 0;
constexpr int L_TQ = 10240;
constexpr int L_TK = 12288;
constexpr int L_RED = 16384;

DEVI void gather_mem(rsrc_t ws, int s, unsigned char* lds, int wave, int lane, int ownp) {
  const int u0 = wave * 80 + lane, u1 = wave * 80 + 64 + lane;
  const bool n0 = (u0 >> 4) != ownp, n1 = lane < 16 && (u1 >> 4) != ownp;
  const int base = W_MEM + s * 16384;
  const int o0 = n0 ? base + u0 * 16 : OOB, o1 = n1 ? base + u1 * 16 : OOB;
  v4u d0, d1; unsigned spins = 0;
  for (;;) {
    asm volatile("" ::: "memory");
    d0 = ld16c(ws, o0); d1 = ld16c(ws, o1);
    if (__all(valid4(d0) & valid4(d1))) break;
    if (give_up(spins, ws)) break;
  }
  if (n0) sts16(lds, L_M + u0 * 16, d0);
  if (n1) sts16(lds, L_M + u1 * 16, d1);
}
DEVI int t2_off(int e, int g) {
  const int kk = e >> 6, gm = (e >> 4) & 3, m = (e >> 2) & 3, half = (e >> 1) & 1, t = e & 1;
  return (kk * 32 + (m >> 1) * 16 + gm * 4 + (m & 1) * 2 + t) * 32 + half * 16 + g * 8;
}

DEVI void role_A(const Args& a, int h, unsigned char* lds, int tid) {
  const int lane = tid & 63, wave = __builtin_amdgcn_readfirstlane(tid >> 6);
  const rsrc_t ws = mkrs(a.ws, WS_BYTES);
  bf8 w[2][20];
  load_w256(w, mkrs(a.wq, 640 * 8192), h * 256, mkrs(a.wk, 640 * 8192), h * 256, 8192, 256, lds, tid);
  const int idx = (lane & 8) ? 0xEEEE : 0x4444;
  const int n = lane & 15, g4 = lane >> 4;
  const int e = 32 * (wave & 3) + 8 * (n >> 2) + 4 * (lane >> 5) + (n & 3);
  const int wr_off = wave < 4 ? L_TQ + t2_off(e, g4 & 1) : L_TK + e * 16 + (g4 & 1) * 8;
  const int a4 = n >> 2, c = lane & 3;
  const int qrd = L_TQ + (g4 * 4 + a4) * 32 + c * 8;
  const int krd = L_TK + (8 * g4 + a4) * 16 + (c & 1) * 8;
  int pst;
  if (lane < 32) pst = wave * 128 + (g4 & 1) * 64 + (n >> 3) * 32 + (n & 1) * 16 + ((n & 7) >> 1) * 4;
  else pst = (n < 8 && !(n & 1)) ? 512 + wave * 32 + (g4 & 1) * 16 + (n >> 1) * 4 : -1;
  bf8 kx[2][4];
  unsigned kprog[2] = {0u, 0u};
  if (wave < 4) {
    kprog[0] = wait_prog(ws, C_KP + (h >> 1) * 4, 1, 1u, lane, 1);
#pragma unroll
    for (int i = 0; i < 8; ++i) kx[i >> 2][i & 3] = asbf8(ld16c(ws, W_KX + h * 8192 + i * 1024 + lane * 16));
  } else {
    kprog[0] = wait_prog(ws, C_KP + (h >> 1) * 4, 1, 1u, lane, 1);
  }
  const float cs = 0.08838834764831845f * 1.4426950408889634f;
  for (int s = 0; s < NSEG; ++s) {
    gather_mem(ws, s, lds, wave, lane, -1);
    bar();
    if (wave == 0) STAMP(((0 * 64 + h) * 64 + s) * 4 + 0);
    bf8 af[10];
#pragma unroll
    for (int k = 0; k < 10; ++k) af[k] = asbf8(lds16(lds, L_M + k * 1024 + lane * 16));
    f4 c0 = {0, 0, 0, 0}, c1 = {0, 0, 0, 0};
#pragma unroll
    for (int k = 0; k < 10; ++k) {
      c0 = smfma(af[k], w[0][2 * k], w[0][2 * k + 1], c0, idx);
      c1 = smfma(af[k], w[1][2 * k], w[1][2 * k + 1], c1, idx);
    }
    {
      v2u o; o.x = pk(swapadd32(c0[0], c1[0]), swapadd32(c0[1], c1[1])); o.y = pk(swapadd32(c0[2], c1[2]), swapadd32(c0[3], c1[3]));
      sts8(lds, wr_off, o);
    }
    bar();
    if (wave == 0) STAMP(((0 * 64 + h) * 64 + s) * 4 + 1);
    const int par = (s + 1) & 1;
    if (wave < 4) {
      if (s < NSEG - 1) {
        bf8 q0 = ldtr2(lds, qrd, qrd + 512), q1 = ldtr2(lds, qrd + 1024, qrd + 1536);
        bf8 km[4];
#pragma unroll
        for (int k = 0; k < 4; ++k) km[k] = ldtr2(lds, krd + k * 512, krd + k * 512 + 64);
        f4 d0 = {0, 0, 0, 0}, d1 = d0, d2 = d0;
        d1 = smfma(q0, kx[0][0], kx[0][1], d1, idx); d2 = smfma(q0, kx[1][0], kx[1][1], d2, idx);
        d1 = smfma(q1, kx[0][2], kx[0][3], d1, idx); d2 = smfma(q1, kx[1][2], kx[1][3], d2, idx);
        d0 = smfma(q0, km[0], km[1], d0, idx); d0 = smfma(q1, km[2], km[3], d0, idx);
        float s0, s1, s2;
        if (wave == 0) { s0 = d0[0]; s1 = d1[0]; s2 = d2[0]; }
        else if (wave == 1) { s0 = d0[1]; s1 = d1[1]; s2 = d2[1]; }
        else if (wave == 2) { s0 = d0[2]; s1 = d1[2]; s2 = d2[2]; }
        else { s0 = d0[3]; s1 = d1[3]; s2 = d2[3]; }
        s1 = swapadd32(s1, s1); s2 = swapadd32(s2, s2); s0 = swapadd32(s0, s0);
        float m = row16_max(fmaxf(fmaxf(s1, s2), s0));
        const float mc = -m * cs;
        float p1 = __builtin_amdgcn_exp2f(fmaf(s1, cs, mc)), p2 = __builtin_amdgcn_exp2f(fmaf(s2, cs, mc)), p0 = __builtin_amdgcn_exp2f(fmaf(s0, cs, mc));
        float l = row16_sum(fmaf(p0, 0.5f, p1 + p2));
        const float r = __builtin_amdgcn_rcpf(l);
        p0 *= r; p1 *= r; p2 *= r;
        const float p0n = dpp<0xB1>(p0);
        unsigned dv = lane < 32 ? pk(p1, p2) : pk(p0, p0n);
        st4c(ws, pst >= 0 ? W_P + s * 32768 + h * 640 + pst : OOB, nosent(dv));
        if (wave == 0) STAMP(((0 * 64 + h) * 64 + s) * 4 + 2);
        if (s + 1 < NSEG - 1) {
          if (kprog[par] < (unsigned)(s + 2)) kprog[par] = wait_prog(ws, C_KP + par * 64 + (h >> 1) * 4, 1, (unsigned)(s + 2), lane, 1);
#pragma unroll
          for (int i = 0; i < 8; ++i) kx[i >> 2][i & 3] = asbf8(ld16c(ws, W_KX + ((s + 1) * 32 + h) * 8192 + i * 1024 + lane * 16));
        }
      }
    } else {
      const int k = wave - 4;
      v4u kv = __builtin_bit_cast(v4u, ldtr2(lds, krd + k * 512, krd + k * 512 + 64));
      kv.x = nosent(kv.x); kv.y = nosent(kv.y); kv.z = nosent(kv.z); kv.w = nosent(kv.w);
      st16c(ws, n < 8 ? W_KM + (s * 32 + h) * 2048 + k * 512 + g4 * 128 + n * 16 : OOB, kv);
      if (s + 1 < NSEG && kprog[par] < (unsigned)(s + 2)) kprog[par] = wait_prog(ws, C_KP + par * 64 + (h >> 1) * 4, 1, (unsigned)(s + 2), lane, 1);
    }
  }
}

template <bool VO>
DEVI void role_S2(const Args& a, int t, unsigned char* lds, int tid) {
  const int lane = tid & 63, wave = __builtin_amdgcn_readfirstlane(tid >> 6);
  const rsrc_t ws = mkrs(a.ws, WS_BYTES);
  bf8 wv[20], wo[5][4];
  if (VO) load_wvo(wv, wo, a, t, lds, tid);
  const int idx = (lane & 8) ? 0xEEEE : 0x4444;
  const int n = lane & 15, g4 = lane >> 4;
  const int e = 32 * (wave >> 1) + 8 * (n >> 2) + 4 * (wave & 1) + (n & 3);
  const int wr_off = L_TQ + t2_off(e, g4 & 1);
  const int a4 = n >> 2, c = lane & 3;
  const int qrd = L_TQ + (g4 * 4 + a4) * 32 + c * 8;
  const int r8 = lane & 7, hf = (lane >> 3) & 1;
  const int pt_off = (4 * wave + (g4 >> 1)) * 640 + (r8 & 3) * 128 + (r8 >> 2) * 64 + (g4 & 1) * 32 + hf * 16;
  const int pm_off = n < 8 ? (4 * wave + g4) * 640 + 512 + (n & 3) * 32 + (n >> 2) * 16 : -1;
  const int u_off = (t * 32 + 4 * wave) * 256 + lane * 16;
  const int z_off = ((4 * wave) * 640 + 16 * t + n) * 64 + g4 * 16;
  bf8 z[4], zn[4];
  unsigned zprog[2] = {0u, 0u};
  zprog[0] = wait_prog(ws, C_ZP, 32, 1u, lane, 1);
#pragma unroll
  for (int i = 0; i < 4; ++i) z[i] = asbf8(ld16c(ws, W_Z + z_off + i * 40960));
  for (int s = 0; s < NSEG; ++s) {
    const int par = (s + 1) & 1;
    if (VO) {
      gather_mem(ws, s, lds, wave, lane, s == 0 ? -1 : t);
      bar();
    }
    if (wave == 0) STAMP(((1 * 64 + t) * 64 + s) * 4 + 0);
    if (s + 1 < NSEG) {
      if (zprog[par] < (unsigned)(s + 2)) zprog[par] = wait_prog(ws, C_ZP + par * 128, 32, (unsigned)(s + 2), lane, 1);
      if (s + 1 < NSEG - 1) {
#pragma unroll
        for (int i = 0; i < 4; ++i) zn[i] = asbf8(ld16c(ws, W_Z + (s + 1) * (32 * 40960) + z_off + i * 40960));
      }
    }
    if (VO) {
      bf8 af[10];
#pragma unroll
      for (int k = 0; k < 10; ++k) af[k] = asbf8(lds16(lds, L_M + k * 1024 + lane * 16));
      f4 c0 = {0, 0, 0, 0};
#pragma unroll
      for (int k = 0; k < 10; ++k) c0 = smfma(af[k], wv[2 * k], wv[2 * k + 1], c0, idx);
      {
        v2u o; o.x = pk(swapadd32(c0[0], c0[0]), swapadd32(c0[1], c0[1])); o.y = pk(swapadd32(c0[2], c0[2]), swapadd32(c0[3], c0[3]));
        if (lane < 32) sts8(lds, wr_off, o);
      }
      bar();
      bf8 v0 = ldtr2(lds, qrd, qrd + 512), v1 = ldtr2(lds, qrd + 1024, qrd + 1536);
      f4 u[5];
#pragma unroll
      for (int i = 0; i < 5; ++i) { u[i] = (f4){0, 0, 0, 0}; u[i] = smfma(v0, wo[i][0], wo[i][1], u[i], idx); u[i] = smfma(v1, wo[i][2], wo[i][3], u[i], idx); }
#pragma unroll
      for (int p = 0; p < 3; ++p) {
        const int i0 = 2 * p, i1 = p < 2 ? 2 * p + 1 : 4;
        v2u o; o.x = nosent(pk(swapadd32(u[i0][0], u[i1][0]), swapadd32(u[i0][1], u[i1][1])));
        o.y = nosent(pk(swapadd32(u[i0][2], u[i1][2]), swapadd32(u[i0][3], u[i1][3])));
        const int tt = 5 * wave + i0 + (lane >> 5);
        const bool ok = p < 2 || lane < 32;
        st8c(ws, ok ? W_U + s * 327680 + (tt * 32 + t) * 256 + n * 16 + (g4 & 1) * 8 : OOB, o);
      }
      if (wave == 0) STAMP(((1 * 64 + t) * 64 + s) * 4 + 1);
    }
    if (s == NSEG - 1) break;
    {
      const int pb = W_P + s * 32768;
      v4u p0, p1, pm, uu; unsigned spins = 0;
      for (;;) {
        asm volatile("" ::: "memory");
        uu = ld16c(ws, W_U + s * 327680 + u_off);
        p0 = ld16c(ws, pb + pt_off); p1 = ld16c(ws, pb + pt_off + 1280);
        pm = ld16c(ws, pm_off >= 0 ? pb + pm_off : OOB);
        if (__all(valid4(p0) & valid4(p1) & valid4(pm) & valid4(uu))) break;
        if (give_up(spins, ws)) break;
      }
      if (wave == 0) STAMP(((1 * 64 + t) * 64 + s) * 4 + 2);
      f4 c1 = {0, 0, 0, 0}, c2 = {0, 0, 0, 0};
      c1 = smfma(asbf8(p0), z[0], z[1], c1, idx);
      c2 = mfma(asbf8(pm), asbf8(uu), c2);
      c1 = smfma(asbf8(p1), z[2], z[3], c1, idx);
      c1 += c2;
      stsf4(lds, L_RED + wave * 1024 + lane * 16, c1);
      bar();
      if (wave == 0) {
        f4 r = ldsf4(lds, L_RED + lane * 16);
#pragma unroll
        for (int i = 1; i < 8; ++i) r += ldsf4(lds, L_RED + i * 1024 + lane * 16);
        float r0 = swapadd32(r[0], r[0]), r1 = swapadd32(r[1], r[1]), r2 = swapadd32(r[2], r[2]), r3 = swapadd32(r[3], r[3]);
        const bool up = lane >= 32, odd = n & 1;
        const float ea = up ? r2 : r0, eb = up ? r3 : r1;
        const float give = odd ? ea : eb;
        const float got = dpp<0xB1>(give);
        const float mine = odd ? eb : ea;
        const unsigned dv = nosent(odd ? pk(got, mine) : pk(mine, got));
        const int row = 4 * (g4 & 1) + 2 * (lane >> 5) + (n & 1), p = n >> 1;
        const int mo = t * 256 + (p & 1) * 128 + row * 16 + (p >> 1) * 4;
        st4c(ws, W_MEM + (s + 1) * 16384 + mo, dv);
        if (VO) sts4(lds, L_M + mo, dv);
        STAMP(((1 * 64 + t) * 64 + s) * 4 + 3);
      }
#pragma unroll
      for (int i = 0; i < 4; ++i) z[i] = zn[i];
    }
  }
}

constexpr int L_X0 = 0, L_X1 = 40960, L_AUX = 81920;
DEVI void xn_fetch(v4u (&st)[5], rsrc_t ws, int s, int tid) {
#pragma unroll
  for (int j = 0; j < 5; ++j) st[j] = ld16(ws, s < NSEG ? W_XN + s * 40960 + (j * 512 + tid) * 16 : OOB);
}
DEVI void xn_put(const v4u (&st)[5], unsigned char* lds, int base, int tid) {
#pragma unroll
  for (int j = 0; j < 5; ++j) sts16(lds, base + (j * 512 + tid) * 16, st[j]);
}
DEVI void gemm256(f4 (&acc)[2][2], const bf8 (&w)[2][20], const unsigned char* lds, int xb, int lane) {
#pragma unroll
  for (int u = 0; u < 2; ++u) { acc[u][0] = (f4){0, 0, 0, 0}; acc[u][1] = (f4){0, 0, 0, 0}; }
#pragma unroll
  for (int k = 0; k < 20; ++k) {
    bf8 x0 = asbf8(lds16(lds, xb + k * 1024 + lane * 16)), x1 = asbf8(lds16(lds, xb + 20480 + k * 1024 + lane * 16));
    acc[0][0] = mfma(w[0][k], x0, acc[0][0]); acc[1][0] = mfma(w[1][k], x0, acc[1][0]);
    acc[0][1] = mfma(w[0][k], x1, acc[0][1]); acc[1][1] = mfma(w[1][k], x1, acc[1][1]);
  }
}
DEVI v4u pack8(f4 a, f4 b) { v4u o = {pk(a[0], a[1]), pk(a[2], a[3]), pk(b[0], b[1]), pk(b[2], b[3])}; return o; }

template <int MODE>
DEVI void role_W256(const Args& a, int sl, int par, unsigned char* lds, int tid) {
  const int lane = tid & 63, wave = __builtin_amdgcn_readfirstlane(tid >> 6);
  const rsrc_t ws = mkrs(a.ws, WS_BYTES);
  v4u st[5];
  xn_fetch(st, ws, par, tid);
  bf8 w[2][20];
  if (MODE == 0) load_w256s(w, mkrs(a.wk, 640 * 8192), sl * 512, 8192, 256, lds, tid);
  if (MODE == 2) load_w256s(w, mkrs(a.wq, 640 * 8192), sl * 512, 8192, 256, lds, tid);
  if (MODE == 1) load_w256s(w, mkrs(a.w1, 640 * 3456), sl * 512, 3456, 1728 - sl * 256, lds, tid);
  const int n = lane & 15, g4 = lane >> 4;
  v4u bias = {0, 0, 0, 0};
  if (MODE == 1) bias = ld16(mkrs(a.b1, 3456), (sl * 256 + wave * 32 + g4 * 8) * 2);
  xn_put(st, lds, L_X0, tid);
  bar();
  const v4u sent = {SENT, SENT, SENT, SENT};
  unsigned kprog = 0;
  int it = 0;
  for (int s = par; s < NSEG; s += 2, ++it) {
    const int xb = (it & 1) ? L_X1 : L_X0, xo = (it & 1) ? L_X0 : L_X1;
    xn_fetch(st, ws, s + 2, tid);
    f4 acc[2][2];
    gemm256(acc, w, lds, xb, lane);
    if (MODE == 0) {
      const int hd = 2 * sl + (wave >> 2);
#pragma unroll
      for (int rt = 0; rt < 2; ++rt) st16c(ws, W_KX + ((s * 32 + hd) * 8 + rt * 4 + (wave & 3)) * 1024 + lane * 16, pack8(acc[0][rt], acc[1][rt]));
      if (wave < 4) st16c(ws, W_KM + (s * 32 + 2 * sl) * 2048 + tid * 16, sent);
    }
    if (MODE == 1) {
      const int kk = sl * 8 + wave;
#pragma unroll
      for (int rt = 0; rt < 2; ++rt) {
        v4u pv = pack8(acc[0][rt], acc[1][rt]), o;
#pragma unroll
        for (int j = 0; j < 4; ++j) {
          unsigned hb = pk(blo(pv[j]) + blo(bias[j]), bhi(pv[j]) + bhi(bias[j]));
          float lo = blo(hb), hi = bhi(hb);
          lo = lo > 0.f ? lo : 0.f; hi = hi > 0.f ? hi : 0.f;
          o[j] = pk(lo * lo, hi * hi);
        }
        st16c(ws, kk < 54 ? W_H + ((s * 2 + rt) * 54 + kk) * 1024 + lane * 16 : OOB, o);
      }
    }
    if (MODE == 2) {
#pragma unroll
      for (int rt = 0; rt < 2; ++rt) sts16(lds, L_AUX + (((wave >> 2) * 2 + rt) * 4 + (wave & 3)) * 1024 + lane * 16, pack8(acc[0][rt], acc[1][rt]));
      bar();
      if (wave < 4) {
        const int hh = wave >> 1, rt = wave & 1, hd = 2 * sl + hh;
        if (kprog < (unsigned)(s + 1)) kprog = wait_prog(ws, C_KP + par * 64 + sl * 4, 1, (unsigned)(s + 1), lane, 2);
        bf8 kf[2][4], qf[4];
#pragma unroll
        for (int i = 0; i < 8; ++i) kf[i >> 2][i & 3] = asbf8(ld16c(ws, W_KX + (s * 32 + hd) * 8192 + i * 1024 + lane * 16));
#pragma unroll
        for (int k = 0; k < 4; ++k) qf[k] = asbf8(lds16(lds, L_AUX + ((hh * 2 + rt) * 4 + k) * 1024 + lane * 16));
        f4 d0 = {0, 0, 0, 0}, d1 = d0, d2 = d0;
#pragma unroll
        for (int k = 0; k < 4; ++k) { d1 = mfma(kf[0][k], qf[k], d1); d2 = mfma(kf[1][k], qf[k], d2); }
        v4u km[4]; unsigned spins = 0;
        const int ko = n < 8 ? W_KM + (s * 32 + hd) * 2048 + g4 * 128 + n * 16 : OOB;
        for (;;) {
          asm volatile("" ::: "memory");
#pragma unroll
          for (int k = 0; k < 4; ++k) km[k] = ld16c(ws, n < 8 ? ko + k * 512 : OOB);
          if (__all(valid4(km[0]) & valid4(km[1]) & valid4(km[2]) & valid4(km[3]))) break;
          if (give_up(spins, ws)) break;
          __builtin_amdgcn_s_sleep(4);
        }
#pragma unroll
        for (int k = 0; k < 4; ++k) d0 = mfma(asbf8(km[k]), qf[k], d0);
        const float sc = 0.08838834764831845f, NEG = -1.0e9f;
        const int q = 16 * rt + n;
        float v[12];
#pragma unroll
        for (int i = 0; i < 4; ++i) {
          v[i] = g4 < 2 ? rbf(rbf(d0[i]) * sc) : -INFINITY;
          v[4 + i] = (4 * g4 + i <= q) ? rbf(rbf(d1[i]) * sc) : NEG;
          v[8 + i] = (16 + 4 * g4 + i <= q) ? rbf(rbf(d2[i]) * sc) : NEG;
        }
        float m = v[0];
#pragma unroll
        for (int i = 1; i < 12; ++i) m = fmaxf(m, v[i]);
        m = x4_max(m);
        float l = 0.f;
#pragma unroll
        for (int i = 0; i < 12; ++i) { v[i] = __builtin_amdgcn_exp2f((v[i] - m) * 1.4426950408889634f); l += v[i]; }
        l = x4_sum(l);
        const float r = 1.0f / l;
        v4u pt = {pk(v[4] * r, v[8] * r), pk(v[5] * r, v[9] * r), pk(v[6] * r, v[10] * r), pk(v[7] * r, v[11] * r)};
        st16c(ws, W_PT + s * 81920 + (hd * 2 + rt) * 1024 + lane * 16, pt);
        v2u pm = {pk(v[0] * r, v[1] * r), pk(v[2] * r, v[3] * r)};
        st8c(ws, g4 < 2 ? W_PT + s * 81920 + 65536 + ((hd >> 2) * 2 + rt) * 1024 + ((hd & 3) * 16 + n) * 16 + g4 * 8 : OOB, pm);
      }
    }
    xn_put(st, lds, xo, tid);
    drain();
    bar();
    if (tid == 0) st4c(ws, W_CTL + (MODE == 0 ? C_KP + par * 64 : MODE == 1 ? C_HP + par * 32 : C_QP + par * 64) + sl * 4, (unsigned)(s + 1));
  }
}

DEVI void role_VZ(const Args& a, int h, int par, unsigned char* lds, int tid) {
  const int lane = tid & 63, wave = __builtin_amdgcn_readfirstlane(tid >> 6);
  const rsrc_t ws = mkrs(a.ws, WS_BYTES);
  v4u st[5];
  xn_fetch(st, ws, par, tid);
  bf8 wv[20], wo[5][4];
  load_wvo(wv, wo, a, h, lds, tid);
  xn_put(st, lds, L_X0, tid);
  bar();
  const int n = lane & 15, g4 = lane >> 4;
  const v4u sent = {SENT, SENT, SENT, SENT};
  int it = 0;
  for (int s = par; s < NSEG; s += 2, ++it) {
    const int xb = (it & 1) ? L_X1 : L_X0, xo = (it & 1) ? L_X0 : L_X1;
    xn_fetch(st, ws, s + 2, tid);
    f4 v0 = {0, 0, 0, 0}, v1 = v0;
#pragma unroll
    for (int k = 0; k < 20; ++k) {
      bf8 x0 = asbf8(lds16(lds, xb + k * 1024 + lane * 16)), x1 = asbf8(lds16(lds, xb + 20480 + k * 1024 + lane * 16));
      v0 = mfma(wv[k], x0, v0); v1 = mfma(wv[k], x1, v1);
    }
    {
      v2u o0 = {pk(v0[0], v0[1]), pk(v0[2], v0[3])}, o1 = {pk(v1[0], v1[1]), pk(v1[2], v1[3])};
      const int o = L_AUX + (wave >> 1) * 1024 + lane * 16 + (wave & 1) * 8;
      sts8(lds, o, o0); sts8(lds, o + 4096, o1);
    }
    st16c(ws, W_U + s * 327680 + ((tid >> 4) * 32 + h) * 256 + (tid & 15) * 16, sent);
    st16c(ws, tid < 128 ? W_U + s * 327680 + ((32 + (tid >> 4)) * 32 + h) * 256 + (tid & 15) * 16 : OOB, sent);
    bar();
    bf8 vf[2][4];
#pragma unroll
    for (int i = 0; i < 8; ++i) vf[i >> 2][i & 3] = asbf8(lds16(lds, L_AUX + i * 1024 + lane * 16));
#pragma unroll
    for (int t = 0; t < 5; ++t) {
      f4 z0 = {0, 0, 0, 0}, z1 = z0;
#pragma unroll
      for (int k = 0; k < 4; ++k) { z0 = mfma(vf[0][k], wo[t][k], z0); z1 = mfma(vf[1][k], wo[t][k], z1); }
      v4u o = {pk(z0[0], z1[0]), pk(z0[1], z1[1]), pk(z0[2], z1[2]), pk(z0[3], z1[3])};
      st16c(ws, W_Z + ((s * 32 + h) * 640 + 80 * wave + 16 * t + n) * 64 + g4 * 16, o);
    }
    xn_put(st, lds, xo, tid);
    drain();
    bar();
    if (tid == 0) st4c(ws, W_CTL + C_ZP + par * 128 + h * 4, (unsigned)(s + 1));
  }
}

DEVI void role_F2(const Args& a, int cg, int par, unsigned char* lds, int tid) {
  const int lane = tid & 63, wave = __builtin_amdgcn_readfirstlane(tid >> 6);
  const rsrc_t ws = mkrs(a.ws, WS_BYTES);
  const int n = lane & 15, g4 = lane >> 4;
  bf8 w[4][7];
  {
    const rsrc_t r2 = mkrs(a.w2, 1728 * 1280);
    constexpr int RS = 192;
    v4u st[28];
    const int uc = lane & 7, r0 = lane >> 3;
#pragma unroll
    for (int j = 0; j < 28; ++j) st[j] = ld16(r2, (224 * wave + r0 + 8 * j) * 1280 + cg * 128 + uc * 16);
    const int a4 = n >> 2, c = lane & 3;
    const int wb = wave * (2 * 32 * RS);
#pragma unroll
    for (int k = 0; k < 7; ++k) {
      const int base = wb + (k & 1) * 32 * RS;
#pragma unroll
      for (int j = 0; j < 4; ++j) sts16(lds, base + (r0 + 8 * j) * RS + uc * 16, st[k * 4 + j]);
      asm volatile("s_waitcnt lgkmcnt(0)" ::: "memory");
#pragma unroll
      for (int t = 0; t < 4; ++t) { const int o = base + (8 * g4 + a4) * RS + t * 32 + c * 8; w[t][k] = ldtr2(lds, o, o + 4 * RS); }
      asm volatile("" ::: "memory");
    }
    bar();
  }
  const v2u bias = ld8(mkrs(a.b2, 1280), (cg * 64 + (wave & 3) * 16 + g4 * 4) * 2);
  unsigned hprog = 0;
  for (int s = par; s < NSEG; s += 2) {
    if (hprog < (unsigned)(s + 1)) hprog = wait_prog(ws, C_HP + par * 32, 7, (unsigned)(s + 1), lane, 2);
    bf8 hf[2][7];
#pragma unroll
    for (int k = 0; k < 7; ++k)
#pragma unroll
      for (int rt = 0; rt < 2; ++rt) { const int kk = 7 * wave + k; hf[rt][k] = asbf8(ld16c(ws, kk < 54 ? W_H + ((s * 2 + rt) * 54 + kk) * 1024 + lane * 16 : OOB)); }
    f4 acc[2][4];
#pragma unroll
    for (int i = 0; i < 8; ++i) acc[i >> 2][i & 3] = (f4){0, 0, 0, 0};
#pragma unroll
    for (int k = 0; k < 7; ++k)
#pragma unroll
      for (int rt = 0; rt < 2; ++rt)
#pragma unroll
        for (int t = 0; t < 4; ++t) acc[rt][t] = mfma(w[t][k], hf[rt][k], acc[rt][t]);
#pragma unroll
    for (int i = 0; i < 8; ++i) stsf4(lds, (wave * 8 + i) * 1024 + lane * 16, acc[i >> 2][i & 3]);
    bar();
    f4 r = ldsf4(lds, wave * 1024 + lane * 16);
#pragma unroll
    for (int i = 1; i < 8; ++i) r += ldsf4(lds, (i * 8 + wave) * 1024 + lane * 16);
    float o[4];
    const float bb[4] = {blo(bias.x), bhi(bias.x), blo(bias.y), bhi(bias.y)};
#pragma unroll
    for (int i = 0; i < 4; ++i) {
      const float y = rbf(rbf(r[i]) + bb[i]);
      o[i] = y / (1.0f + __expf(-y));
    }
    v2u ov = {pk(o[0], o[1]), pk(o[2], o[3])};
    st8c(ws, W_G + ((s * 10 + cg) * 8 + wave) * 512 + lane * 8, ov);
    drain();
    bar();
    if (tid == 0) st4c(ws, W_CTL + C_GP + par * 64 + cg * 4, (unsigned)(s + 1));
  }
}

DEVI void role_out(const Args& a, unsigned char* lds, int tid) {
  const int lane = tid & 63, wave = __builtin_amdgcn_readfirstlane(tid >> 6);
  const rsrc_t ws = mkrs(a.ws, WS_BYTES);
  const rsrc_t rx = mkrs(a.x, 2048 * 1280), ro = mkrs(a.out, 2048 * 1280);
  const int n = lane & 15, g4 = lane >> 4;
  for (;;) {
    if (tid == 0) {
      unsigned tk = __hip_atomic_fetch_add((GAS unsigned*)(a.ws + W_CTL + C_TASK), 1u, __ATOMIC_RELAXED, __HIP_MEMORY_SCOPE_AGENT);
      sts4(lds, 65536, tk);
    }
    bar();
    const int task = __builtin_amdgcn_readfirstlane((int)*(const LAS unsigned*)(LAS unsigned char*)(lds + 65536));
    if (task >= NSEG * 10) break;
    const int s = task / 10, cg = task - s * 10, par = s & 1;
    wait_prog(ws, C_ZP + par * 128, 32, (unsigned)(s + 1), lane, 4);
    bf8 y[5][4];
#pragma unroll
    for (int hh = 0; hh < 4; ++hh)
#pragma unroll
      for (int t = 0; t < 4; ++t) y[hh][t] = asbf8(ld16c(ws, W_Z + ((s * 32 + 4 * wave + hh) * 640 + cg * 64 + 16 * t + n) * 64 + g4 * 16));
    const int tok = s * 32 + (wave >> 2) * 16 + n;
    const int xo = tok * 1280 + (cg * 64 + (wave & 3) * 16 + g4 * 4) * 2;
    const v2u xv = ld8(rx, xo);
    wait_prog(ws, C_GP + par * 64 + cg * 4, 1, (unsigned)(s + 1), lane, 4);
    const v2u gv = ld8c(ws, W_G + ((s * 10 + cg) * 8 + wave) * 512 + lane * 8);
    wait_prog(ws, C_QP + par * 64, 16, (unsigned)(s + 1), lane, 4);
    bf8 p[5][2];
#pragma unroll
    for (int hh = 0; hh < 4; ++hh)
#pragma unroll
      for (int rt = 0; rt < 2; ++rt) p[hh][rt] = asbf8(ld16c(ws, W_PT + s * 81920 + ((4 * wave + hh) * 2 + rt) * 1024 + lane * 16));
#pragma unroll
    for (int rt = 0; rt < 2; ++rt) p[4][rt] = asbf8(ld16c(ws, W_PT + s * 81920 + 65536 + (wave * 2 + rt) * 1024 + lane * 16));
    {
      v4u uu[4]; unsigned spins = 0;
      for (;;) {
        asm volatile("" ::: "memory");
#pragma unroll
        for (int t = 0; t < 4; ++t) uu[t] = ld16c(ws, W_U + s * 327680 + ((cg * 4 + t) * 32 + 4 * wave) * 256 + lane * 16);
        if (__all(valid4(uu[0]) & valid4(uu[1]) & valid4(uu[2]) & valid4(uu[3]))) break;
        if (give_up(spins, ws)) break;
        __builtin_amdgcn_s_sleep(4);
      }
#pragma unroll
      for (int t = 0; t < 4; ++t) y[4][t] = asbf8(uu[t]);
    }
    f4 acc[2][4];
#pragma unroll
    for (int i = 0; i < 8; ++i) acc[i >> 2][i & 3] = (f4){0, 0, 0, 0};
#pragma unroll
    for (int hh = 0; hh < 5; ++hh)
#pragma unroll
      for (int rt = 0; rt < 2; ++rt)
#pragma unroll
        for (int t = 0; t < 4; ++t) acc[rt][t] = mfma(y[hh][t], p[hh][rt], acc[rt][t]);
#pragma unroll
    for (int i = 0; i < 8; ++i) stsf4(lds, (wave * 8 + i) * 1024 + lane * 16, acc[i >> 2][i & 3]);
    bar();
    f4 r = ldsf4(lds, wave * 1024 + lane * 16);
#pragma unroll
    for (int i = 1; i < 8; ++i) r += ldsf4(lds, (i * 8 + wave) * 1024 + lane * 16);
    const float gg[4] = {blo(gv.x), bhi(gv.x), blo(gv.y), bhi(gv.y)}, xx[4] = {blo(xv.x), bhi(xv.x), blo(xv.y), bhi(xv.y)};
    float o[4];
#pragma unroll
    for (int i = 0; i < 4; ++i) o[i] = xx[i] + rbf(rbf(r[i]) * gg[i]);
    v2u ov = {pk(o[0], o[1]), pk(o[2], o[3])};
    st8(ro, xo, ov);
  }
}

__global__ void __launch_bounds__(512, 2) rmt_main(Args a) {
  extern __shared__ __attribute__((aligned(16))) unsigned char lds[];
  const int tid = threadIdx.x, b = blockIdx.x;
  const int xc = b & 7, ix = b >> 3;
  if (xc < 4 && ix < 18) {
    const int c = ix * 4 + xc;
    if (c < 32) role_A(a, c, lds, tid);
    else if (c < 64) role_S2<true>(a, c - 32, lds, tid);
    else role_S2<false>(a, c - 32, lds, tid);
    return;
  }
  const int wix = xc < 4 ? (ix - 18) * 4 + xc : 56 + ix * 4 + (xc - 4);
  if (wix < 64) role_VZ(a, wix >> 1, wix & 1, lds, tid);
  else if (wix < 96) role_W256<0>(a, (wix - 64) >> 1, wix & 1, lds, tid);
  else if (wix < 128) role_W256<2>(a, (wix - 96) >> 1, wix & 1, lds, tid);
  else if (wix < 142) role_W256<1>(a, (wix - 128) >> 1, wix & 1, lds, tid);
  else if (wix < 162) role_F2(a, (wix - 142) >> 1, wix & 1, lds, tid);
  bar();
  role_out(a, lds, tid);
}

__global__ void __launch_bounds__(512) rmt_pre(Args a) {
  const int tid = threadIdx.x, lane = tid & 63, wave = tid >> 6, b = blockIdx.x;
  const rsrc_t ws = mkrs(a.ws, WS_BYTES);
  const rsrc_t rx = mkrs(a.x, 2048 * 1280);
  const int row = b * 8 + wave;
  v4u d0 = ld16(rx, row * 1280 + lane * 16), d1 = ld16(rx, lane < 16 ? row * 1280 + 1024 + lane * 16 : OOB);
  const int gt = b * 512 + tid;
  {
    const v4u sent = {SENT, SENT, SENT, SENT}, zero = {0, 0, 0, 0};
    if (gt < 63 * 640) { const int sl = gt / 640, u = gt - sl * 640; st16(ws, W_MEM + (sl + 1) * 16384 + u * 16, sent); }
    else if (gt >= 40960 && gt < 122880) { const int i = gt - 40960, sl = i / 1280, u = i - sl * 1280; st16(ws, W_P + sl * 32768 + u * 16, sent); }
    else if (gt >= 122880 && gt < 122880 + 256) st16(ws, W_CTL + (gt - 122880) * 16, zero);
    else if (gt >= 123904 && gt < 123904 + 2560) {
      const int i = gt - 123904, r = i / 320, pp = i - r * 320, t = pp >> 3, p = pp & 7;
      const unsigned v = ((const unsigned*)a.mem0)[r * 320 + pp];
      *(GAS unsigned*)(a.ws + W_MEM + t * 256 + (p & 1) * 128 + r * 16 + (p >> 1) * 4) = nosent(v);
    }
  }
  float v[16];
#pragma unroll
  for (int j = 0; j < 4; ++j) { v[2 * j] = blo(d0[j]); v[2 * j + 1] = bhi(d0[j]); v[8 + 2 * j] = blo(d1[j]); v[9 + 2 * j] = bhi(d1[j]); }
  float sum = 0.f;
#pragma unroll
  for (int j = 0; j < 16; ++j) sum += v[j];
  sum = x4_sum(row16_sum(sum));
  const float mean = sum * (1.0f / 640.0f);
  float sq = 0.f;
#pragma unroll
  for (int j = 0; j < 8; ++j) { const float t = v[j] - mean; sq += t * t; }
  if (lane < 16) {
#pragma unroll
    for (int j = 8; j < 16; ++j) { const float t = v[j] - mean; sq += t * t; }
  }
  sq = x4_sum(row16_sum(sq));
  const float mb = rbf(mean), vb = rbf(sq * (1.0f / 640.0f));
  const float sd = rbf(sqrtf(rbf(vb + 1e-05f)));
  v4u o0, o1;
#pragma unroll
  for (int j = 0; j < 4; ++j) {
    o0[j] = pk(rbf(v[2 * j] - mb) / sd, rbf(v[2 * j + 1] - mb) / sd);
    o1[j] = pk(rbf(v[8 + 2 * j] - mb) / sd, rbf(v[9 + 2 * j] - mb) / sd);
  }
  const int s = row >> 5, rt = (row >> 4) & 1, tn = row & 15;
  const int base = W_XN + (s * 2 + rt) * 20480 + tn * 16;
  st16(ws, base + (lane >> 2) * 1024 + (lane & 3) * 256, o0);
  st16(ws, lane < 16 ? base + (16 + (lane >> 2)) * 1024 + (lane & 3) * 256 : OOB, o1);
}

extern "C" void kernel_launch(void* const* d_in, const int* in_sizes, int n_in, void* d_out, int out_size, void* d_ws, size_t ws_size,
                              hipStream_t stream) {
  Args a;
  a.x = (const uint16_t*)d_in[0]; a.mem0 = (const uint16_t*)d_in[1]; a.wq = (const uint16_t*)d_in[2]; a.wk = (const uint16_t*)d_in[3];
  a.wv = (const uint16_t*)d_in[4]; a.wo = (const uint16_t*)d_in[5]; a.w1 = (const uint16_t*)d_in[8]; a.b1 = (const uint16_t*)d_in[9];
  a.w2 = (const uint16_t*)d_in[10]; a.b2 = (const uint16_t*)d_in[11]; a.out = (uint16_t*)d_out; a.ws = (unsigned char*)d_ws;
  constexpr int LDS_BYTES = 148 * 1024;
  (void)hipFuncSetAttribute((const void*)rmt_main, hipFuncAttributeMaxDynamicSharedMemorySize, LDS_BYTES);
  rmt_pre<<<256, 512, 0, stream>>>(a);
  rmt_main<<<256, 512, LDS_BYTES, stream>>>(a);
}
```

```cpp
#include <hip/hip_runtime.h>
#include <stdint.h>

typedef unsigned v4u __attribute__((ext_vector_type(4)));
typedef unsigned v2u __attribute__((ext_vector_type(2)));
typedef float f4 __attribute__((ext_vector_type(4)));
typedef float f2 __attribute__((ext_vector_type(2)));
typedef __bf16 bf2 __attribute__((ext_vector_type(2)));
typedef __bf16 bf8 __attribute__((ext_vector_type(8)));
typedef __bf16 bf16v __attribute__((ext_vector_type(16)));
typedef unsigned v8u __attribute__((ext_vector_type(8)));
typedef short s4 __attribute__((ext_vector_type(4)));
#define LAS __attribute__((address_space(3)))
#define GAS __attribute__((address_space(1)))
#define DEVI __device__ __forceinline__

constexpr int NSEG = 64;
constexpr unsigned SENT = 0xFFFFFFFFu;
constexpr int OOB = (int)0x80000000u;
constexpr int MB = 1 << 20;
constexpr int W_CTL = 0;
constexpr int W_CAL = 4096;
constexpr int W_MEM = 2 * MB + 8192;
constexpr int W_TS  = W_MEM + 20480;
constexpr int W_P   = 4 * MB;
constexpr int W_U   = 8 * MB;
constexpr int W_XN  = 72 * MB;
constexpr int W_G   = 75 * MB;
constexpr int W_KM  = 78 * MB;
constexpr int W_PT  = 82 * MB;
constexpr int W_H   = 88 * MB;
constexpr int W_KX  = 96 * MB;
constexpr int W_Z   = 112 * MB;
constexpr int W_DBG = 200 * MB;
constexpr int WS_BYTES = 240 * MB;
constexpr int C_KP = 0;
constexpr int C_ZP = 256;
constexpr int C_QP = 512;
constexpr int C_HP = 768;
constexpr int C_GP = 1024;
constexpr int C_TASK = 1280;
constexpr int C_ERR = 1536;
constexpr int C_XP = 1792;
constexpr unsigned SPIN_CAP = 1u << 18;

constexpr int D_TS = 704, D_R0 = 0, D_R10 = 1000, D_X0 = 704, D_Y0 = 1150, D_Z0 = 0, D_NC = 4, D_MU = 448;
constexpr int D_PER = 1632, D_GA = 10, D_GV = 0, D_P = 1056, D_U = 1024, D_R1 = 5, D_X = 560, D_Y = 704, D_Z = 15, D_S0 = 24, D_Q1 = 3200, D_G0 = 3, D_G1 = 300, D_G2 = 10, D_G3 = 480, D_A0 = 2200, D_A1 = 3203, D_A2 = 64, D_B0 = 64;
struct Args {
  const uint16_t* x; const uint16_t* mem0; const uint16_t* wq; const uint16_t* wk; const uint16_t* wv; const uint16_t* wo;
  const uint16_t* w1; const uint16_t* b1; const uint16_t* w2; const uint16_t* b2; uint16_t* out; unsigned char* ws;
};

DEVI int sprd(int o) { return o + (o & ~8191); }
DEVI int p_base(int s) { return W_P + s * 65536 + ((s & 32) << 8); }
DEVI int u_base(int s) { return W_U + s * MB + ((s & 2) << 12); }
typedef __amdgpu_buffer_rsrc_t rsrc_t;
DEVI rsrc_t mkrs(const void* p, int bytes) { return __builtin_amdgcn_make_buffer_rsrc((void*)p, 0, bytes, 0x00020000); }
DEVI v4u ld16c(rsrc_t r, int off) { return __builtin_amdgcn_raw_buffer_load_b128(r, off, 0, 16); }
DEVI v4u ld16cs(rsrc_t r, int voff, int soff) { return __builtin_amdgcn_raw_buffer_load_b128(r, voff, soff, 16); }
DEVI void st16cs(rsrc_t r, int voff, int soff, v4u v) { __builtin_amdgcn_raw_buffer_store_b128(v, r, voff, soff, 16); }
DEVI v4u ld16(rsrc_t r, int off) { return __builtin_amdgcn_raw_buffer_load_b128(r, off, 0, 0); }
DEVI v2u ld8c(rsrc_t r, int off) { return __builtin_amdgcn_raw_buffer_load_b64(r, off, 0, 16); }
DEVI v2u ld8(rsrc_t r, int off) { return __builtin_amdgcn_raw_buffer_load_b64(r, off, 0, 0); }
DEVI unsigned ld4cs(rsrc_t r, int voff, int soff) { return __builtin_amdgcn_raw_buffer_load_b32(r, voff, soff, 16); }
DEVI unsigned ld4c(rsrc_t r, int off) { return __builtin_amdgcn_raw_buffer_load_b32(r, off, 0, 16); }
DEVI void st16c(rsrc_t r, int off, v4u v) { __builtin_amdgcn_raw_buffer_store_b128(v, r, off, 0, 16); }
DEVI void st8cs(rsrc_t r, int voff, int soff, v2u v) { __builtin_amdgcn_raw_buffer_store_b64(v, r, voff, soff, 16); }
DEVI void st4cs(rsrc_t r, int voff, int soff, unsigned v) { __builtin_amdgcn_raw_buffer_store_b32(v, r, voff, soff, 16); }
DEVI void st8c(rsrc_t r, int off, v2u v) { __builtin_amdgcn_raw_buffer_store_b64(v, r, off, 0, 16); }
DEVI void st4c(rsrc_t r, int off, unsigned v) { __builtin_amdgcn_raw_buffer_store_b32(v, r, off, 0, 16); }
DEVI void st8(rsrc_t r, int off, v2u v) { __builtin_amdgcn_raw_buffer_store_b64(v, r, off, 0, 0); }
DEVI void st16(rsrc_t r, int off, v4u v) { __builtin_amdgcn_raw_buffer_store_b128(v, r, off, 0, 0); }

DEVI unsigned pk(float lo, float hi) { f2 v = {lo, hi}; return __builtin_bit_cast(unsigned, __builtin_convertvector(v, bf2)); }
DEVI float blo(unsigned u) { return __builtin_bit_cast(float, u << 16); }
DEVI float bhi(unsigned u) { return __builtin_bit_cast(float, u & 0xFFFF0000u); }
DEVI float rbf(float x) { return blo(pk(x, x)); }
DEVI unsigned nosent(unsigned u) { return u < 0xFFFFFFFEu ? u : 0xFFFFFFFEu; }
DEVI unsigned umax(unsigned a, unsigned b) { return a > b ? a : b; }
DEVI unsigned mx4(v4u d) { return umax(umax(d.x, d.y), umax(d.z, d.w)); }
DEVI bool valid4(v4u d) { return mx4(d) != SENT; }
DEVI unsigned now() { return (unsigned)__builtin_amdgcn_s_memtime(); }
DEVI unsigned rt8() { return (unsigned)__builtin_amdgcn_s_memrealtime() << 3; }
DEVI unsigned tmax(unsigned x, unsigned y) { return (int)(x - y) > 0 ? x : y; }
DEVI void wait_rt(unsigned t) {
  int d;
  t = __builtin_amdgcn_readfirstlane(t);
  const unsigned tt = (t >> 3) & ~3u, fine = (t >> 2) & 7u;
#define RT_D "s_memrealtime vcc\n\ts_waitcnt lgkmcnt(0)\n\ts_sub_i32 %0, %1, vcc_lo\n\ts_bfe_i32 %0, %0, 0x1d0000\n\t"
  asm volatile(
      RT_D "s_cmpk_gt_u32 %0, 0xea6\n\ts_cbranch_scc1 4f\n\t"
      "s_cmpk_lt_i32 %0, 8\n\ts_cbranch_scc1 2f\n"
      "1:\n\ts_sleep 1\n\t" RT_D "s_cmpk_gt_i32 %0, 7\n\ts_cbranch_scc1 1b\n"
      "2:\n\t" RT_D "s_cmpk_gt_i32 %0, 0\n\ts_cbranch_scc1 2b\n\t"
      "s_mov_b32 %0, %2\n"
      "3:\n\ts_sub_i32 %0, %0, 1\n\ts_cmpk_gt_i32 %0, -1\n\ts_cbranch_scc1 3b\n"
      "4:"
      : "=&s"(d) : "s"(tt), "s"(fine) : "scc", "vcc", "memory");
}
DEVI void nosched() { __builtin_amdgcn_sched_barrier(0); }
DEVI bf8 asbf8(v4u v) { return __builtin_bit_cast(bf8, v); }
DEVI bf16v cat16(bf8 a, bf8 b) { return __builtin_shufflevector(a, b, 0, 1, 2, 3, 4, 5, 6, 7, 8, 9, 10, 11, 12, 13, 14, 15); }
DEVI f4 mfma(bf8 a, bf8 b, f4 c) { return __builtin_amdgcn_mfma_f32_16x16x32_bf16(a, b, c, 0, 0, 0); }
DEVI f4 smfma(bf8 a, bf8 b0, bf8 b1, f4 c, int idx) { return __builtin_amdgcn_smfmac_f32_16x16x64_bf16(a, cat16(b0, b1), c, idx, 0, 0); }
DEVI void bar() { asm volatile("" ::: "memory"); __builtin_amdgcn_s_waitcnt(0xC07F); asm volatile("s_barrier" ::: "memory"); }
DEVI void drain() { asm volatile("s_waitcnt vmcnt(0)" ::: "memory"); }
DEVI v4u lds16(const unsigned char* l, int off) { return *(const LAS v4u*)(LAS const unsigned char*)(l + off); }
DEVI f4 ldsf4(const unsigned char* l, int off) { return *(const LAS f4*)(LAS const unsigned char*)(l + off); }
DEVI void sts16(unsigned char* l, int off, v4u v) { *(LAS v4u*)(LAS unsigned char*)(l + off) = v; }
DEVI void stsf4(unsigned char* l, int off, f4 v) { *(LAS f4*)(LAS unsigned char*)(l + off) = v; }
DEVI void sts8(unsigned char* l, int off, v2u v) { *(LAS v2u*)(LAS unsigned char*)(l + off) = v; }
DEVI void sts4(unsigned char* l, int off, unsigned v) { *(LAS unsigned*)(LAS unsigned char*)(l + off) = v; }
DEVI v2u ldtr(const unsigned char* l, int off) {
  return __builtin_bit_cast(v2u, __builtin_amdgcn_ds_read_tr16_b64_v4i16((LAS s4*)(LAS unsigned char*)(l + off)));
}
DEVI bf8 ldtr2(const unsigned char* l, int off0, int off1) { v2u a = ldtr(l, off0), b = ldtr(l, off1); v4u v = {a.x, a.y, b.x, b.y}; return asbf8(v); }
DEVI float u2f(unsigned u) { return __builtin_bit_cast(float, u); }
DEVI float swapadd32(float a, float b) {
  auto r = __builtin_amdgcn_permlane32_swap(__builtin_bit_cast(unsigned, a), __builtin_bit_cast(unsigned, b), false, false);
  return u2f(r[0]) + u2f(r[1]);
}
template <int CTRL> DEVI float dpp(float x) { return __builtin_bit_cast(float, __builtin_amdgcn_mov_dpp(__builtin_bit_cast(int, x), CTRL, 0xf, 0xf, true)); }
template <int CTRL> DEVI unsigned dppu(unsigned x) { return (unsigned)__builtin_amdgcn_mov_dpp((int)x, CTRL, 0xf, 0xf, true); }
template <int CTRL, int BANK> DEVI unsigned dppm(unsigned old, unsigned x) { return (unsigned)__builtin_amdgcn_update_dpp((int)old, (int)x, CTRL, 0xf, BANK, false); }
DEVI v2u swap16(unsigned a, unsigned b) { auto r = __builtin_amdgcn_permlane16_swap(a, b, false, false); v2u o; o.x = r[0]; o.y = r[1]; return o; }
DEVI float row16_max(float x) { x = fmaxf(x, dpp<0x128>(x)); x = fmaxf(x, dpp<0x124>(x)); x = fmaxf(x, dpp<0x122>(x)); x = fmaxf(x, dpp<0x121>(x)); return x; }
DEVI float row16_max2(float x) {
  asm("s_nop 1\n\tv_max_f32_dpp %0, %0, %0 row_ror:8 row_mask:0xf bank_mask:0xf\n\ts_nop 1\n\tv_max_f32_dpp %0, %0, %0 row_ror:4 row_mask:0xf bank_mask:0xf\n\t"
      "s_nop 1\n\tv_max_f32_dpp %0, %0, %0 row_ror:2 row_mask:0xf bank_mask:0xf\n\ts_nop 1\n\tv_max_f32_dpp %0, %0, %0 row_ror:1 row_mask:0xf bank_mask:0xf" : "+v"(x));
  return x;
}
DEVI float row16_sum(float x) { x += dpp<0x128>(x); x += dpp<0x124>(x); x += dpp<0x122>(x); x += dpp<0x121>(x); return x; }
DEVI float x4_max(float x) {
  auto s = __builtin_amdgcn_permlane16_swap(__builtin_bit_cast(unsigned, x), __builtin_bit_cast(unsigned, x), false, false);
  x = fmaxf(u2f(s[0]), u2f(s[1]));
  auto t = __builtin_amdgcn_permlane32_swap(__builtin_bit_cast(unsigned, x), __builtin_bit_cast(unsigned, x), false, false);
  return fmaxf(u2f(t[0]), u2f(t[1]));
}
DEVI float x4_sum(float x) {
  auto s = __builtin_amdgcn_permlane16_swap(__builtin_bit_cast(unsigned, x), __builtin_bit_cast(unsigned, x), false, false);
  x = u2f(s[0]) + u2f(s[1]);
  auto t = __builtin_amdgcn_permlane32_swap(__builtin_bit_cast(unsigned, x), __builtin_bit_cast(unsigned, x), false, false);
  return u2f(t[0]) + u2f(t[1]);
}

#define STAMP(slot) do {} while (0)
#define FAILCNT(k) do {} while (0)
#define FAILOUT(slot) do {} while (0)

#define PH(k) do {} while (0)
#define PHDECL do {} while (0)
#define PHOUT(slot) do {} while (0)
#define PHC() do {} while (0)
#define PHW(k) do {} while (0)
#define PHWDECL do {} while (0)
#define PHWOUT(x) do {} while (0)
DEVI bool give_up(unsigned& spins, rsrc_t ws) {
  ++spins;
  if (spins > SPIN_CAP) { st4c(ws, W_CTL + C_ERR, 1u); return true; }
  if ((spins & 255u) == 0u) { unsigned e = ld4c(ws, W_CTL + C_ERR); if (__builtin_amdgcn_readfirstlane(e) != 0u) return true; }
  return false;
}
DEVI unsigned wait_prog(rsrc_t ws, int off, int n, unsigned need, int lane, int slp) {
  unsigned spins = 0, mn;
  const int o = lane < n ? W_CTL + off + lane * 4 : OOB;
  for (;;) {
    asm volatile("" ::: "memory");
    unsigned v = ld4c(ws, o);
    v = lane < n ? v : 0xFFFFu;
    bool ok = v >= need;
    if (__all(ok)) {
      v = min(v, dppu<0x128>(v)); v = min(v, dppu<0x124>(v)); v = min(v, dppu<0x122>(v)); v = min(v, dppu<0x121>(v));
      const v2u sw = swap16(v, v);
      v = min(sw.x, sw.y);
      mn = __builtin_amdgcn_readfirstlane(v);
      break;
    }
    if (give_up(spins, ws)) { mn = 0xFFFFu; break; }
    for (int i = 0; i < slp; ++i) __builtin_amdgcn_s_sleep(2);
  }
  return mn;
}

DEVI void wait_far(rsrc_t ws, int off, int n, unsigned need, unsigned step, int lane) {
  unsigned spins = 0;
  const int o = lane < n ? W_CTL + off + lane * 4 : OOB;
  for (;;) {
    asm volatile("" ::: "memory");
    unsigned v = ld4c(ws, o);
    v = lane < n ? v : 0xFFFFu;
    if (__all(v >= need)) break;
    if (give_up(spins, ws)) break;
    if (__all(v + step >= need)) __builtin_amdgcn_s_sleep(3); else wait_rt(rt8() + 1216u);
  }
}

DEVI void bar0() { asm volatile("s_barrier" ::: "memory"); }
template <int N> struct IC { static constexpr int v = N; };
template <class F>
DEVI void load_w256(bf8 (&w)[2][20], rsrc_t r, int o0, int step, int r0, int uc, unsigned char* lds, int tid, F mid) {
  const int lane = tid & 63, wave = __builtin_amdgcn_readfirstlane(tid >> 6);
  constexpr int RS = 576, CH = 128 * RS, AHEAD = 3;
  v4u st[40];
  const int g = lane >> 4, a4 = (lane & 15) >> 2, c = lane & 3;
  const int rb = (8 * g + a4) * RS + wave * 64 + c * 16;
#pragma unroll
  for (int ch = -AHEAD; ch < 5; ++ch) {
    if (ch + AHEAD < 5) {
#pragma unroll
      for (int j = (ch + AHEAD) * 8; j < (ch + AHEAD) * 8 + 8; ++j) st[j] = __builtin_amdgcn_raw_buffer_load_b128(r, o0, j * step, 0);
    }
    if (ch < 0) { bar0(); continue; }
    const int base = (ch & 1) * CH;
#pragma unroll
    for (int j = 0; j < 8; ++j) sts16(lds, base + (r0 + 16 * j) * RS + uc * 16, st[ch * 8 + j]);
    if (ch >= 3) mid(ch - 3);
    bar();
#pragma unroll
    for (int k = 0; k < 4; ++k)
#pragma unroll
      for (int u = 0; u < 2; ++u) {
        const int o = base + rb + k * 32 * RS + u * 8;
        w[u][ch * 4 + k] = ldtr2(lds, o, o + 4 * RS);
      }
  }
  bar();
}
template <int NT, class F>
DEVI void load_wvo(bf8 (&wv)[20], bf8 (&wo)[NT][4], const Args& a, int h, int tile0, bool flip, unsigned char* lds, int tid, F mid) {
  const int lane = tid & 63, wave = __builtin_amdgcn_readfirstlane(tid >> 6);
  const rsrc_t rv = mkrs(a.wv, 640 * 4096 * 2), ro = mkrs(a.wo, 640 * 4096 * 2);
  const int g = lane >> 4, a4 = (lane & 15) >> 2, c = lane & 3;
  constexpr int RV = 320, RO = 1280 + 64, CH = 160 * RV, AHEAD = 4;
  const int ov = (tid >> 4) * 8192 + h * 256 + (tid & 15) * 16, oo = h * 163840 + tid * 16;
  const int lv = (tid >> 4) * RV + (tid & 15) * 16;
  const int rbv = (8 * g + a4) * RV + (wave >> 1) * 64 + c * 16 + (wave & 1) * 8, rbo = (8 * g + a4) * RO + tile0 * 32 + c * 8;
  auto run = [&](auto fc) __attribute__((always_inline)) {
    constexpr int FL = decltype(fc)::v;
    v4u st[40];
#pragma unroll
    for (int i = -AHEAD; i < 8; ++i) {
      if (i + AHEAD < 8) {
        const int q = (i + AHEAD) ^ FL;
#pragma unroll
        for (int j = q * 5; j < q * 5 + 5; ++j)
          { if (q < 4) st[j] = __builtin_amdgcn_raw_buffer_load_b128(rv, ov, j * 32 * 8192, 0); else st[j] = __builtin_amdgcn_raw_buffer_load_b128(ro, oo, (j - 20) * 8192, 0); }
      }
      if (i < 0) { bar0(); continue; }
      const int q = i ^ FL, base = (i & 1) * CH;
#pragma unroll
      for (int j = 0; j < 5; ++j) {
        if (q < 4) sts16(lds, base + lv + 32 * j * RV, st[q * 5 + j]);
        else { const int u = j * 512 + tid, row = u / 80, uc = u - row * 80; sts16(lds, base + row * RO + uc * 16, st[q * 5 + j]); }
      }
      if (i >= 6) mid(i - 6);
      bar();
      if (q < 4) {
#pragma unroll
        for (int k = 0; k < 5; ++k) { const int o = base + rbv + k * 32 * RV; wv[(q & 3) * 5 + k] = ldtr2(lds, o, o + 4 * RV); }
      } else {
#pragma unroll
        for (int t = 0; t < NT; ++t) { const int o = base + rbo + t * 32; wo[t][q & 3] = ldtr2(lds, o, o + 4 * RO); }
      }
    }
    asm volatile("" :: "i"(FL) : "memory");
    bar();
  };
  if (flip) run(IC<4>()); else run(IC<0>());
}

constexpr int L_M = 0;
constexpr int L_TQ = 10240;
constexpr int L_TK = 12288;
constexpr int L_RED = 16384;
constexpr int L_DUMP = 24576;

struct Gat { int o0, o1, l0, l1; };
DEVI int mem_lds(int u) { return L_M + (u >> 4) * 256 + (u & 1) * 128 + (((u >> 1) & 1) * 4 + ((u >> 3) & 1) * 2 + ((u >> 2) & 1)) * 16; }
DEVI Gat gat_init(int wave, int lane, int ownp) {
  const int u0 = (wave - 2) * 64 + lane, u1 = (wave + 2) * 64 + lane;
  const bool n0 = (u0 >> 3) != ownp, n1 = wave >= 4 && (u1 >> 3) != ownp;
  Gat g;
  g.o0 = n0 ? u0 * 16 : OOB; g.o1 = n1 ? sprd(u1 * 16) : OOB;
  g.l0 = n0 ? mem_lds(u0) : L_DUMP + lane * 16; g.l1 = n1 ? mem_lds(u1) : L_DUMP + lane * 16;
  return g;
}
DEVI int x1(const Args& a, int s) { return -(s >= D_B0 ? D_GV >> 8 : 0); }
struct Sch { unsigned tp; int sc; };
DEVI int x2(const Args& a, int s, const Sch& h) { return D_PER + (h.sc > 0 ? 32 : 0) + (h.sc > D_R1 ? 32 : 0) + (s < 6 ? D_A2 : 0) - (s >= D_B0 ? D_GV & 255 : 0); }
DEVI unsigned ts_load(rsrc_t ws, int s, int lane) { return __builtin_amdgcn_raw_buffer_load_b32(ws, (lane & 15) * 64 + (lane < 16 ? 0 : OOB), W_TS + s * 32768, 16); }
DEVI unsigned ts_get(rsrc_t ws, int s, int lane, unsigned v) {
  if (!__all(v != 0u || lane >= 16)) {
    unsigned spins = 0;
    for (;;) {
      __builtin_amdgcn_s_sleep(4);
      asm volatile("" ::: "memory");
      v = ts_load(ws, s, lane);
      if (__all(v != 0u || lane >= 16)) break;
      if (give_up(spins, ws)) break;
    }
  }
  const unsigned ref = __builtin_amdgcn_readfirstlane(v);
  int d = (int)(v - ref);
  d = max(d, (int)dppu<0x128>(d)); d = max(d, (int)dppu<0x124>(d)); d = max(d, (int)dppu<0x122>(d)); d = max(d, (int)dppu<0x121>(d));
  return (ref + (unsigned)__builtin_amdgcn_readfirstlane(d)) & ~31u;
}
DEVI unsigned ts_ref(const Args& a, rsrc_t ws, int s, int lane, unsigned tsv, unsigned tloc, unsigned T, Sch& h) {
  if (s == 0) return (tloc - 4096u) & ~31u;
  unsigned t = ts_get(ws, s, lane, tsv);
  h.sc = s > 1 && (int)(t - h.tp) > D_PER + 256 ? min(h.sc + D_R1, 3 * D_R1) : max(h.sc - 1, 0);
  h.tp = t;
  if (s > 1) t = tmax(t, T);
  while ((int)(tloc - t) > 0) t += D_MU;
  return t;
}
DEVI void gather_wait(const Args& a, rsrc_t ws, int s, int lane, unsigned& T) {
  if (s == 1) { wait_rt(T - 352u); T = tmax(T, ts_get(ws, s, lane, ts_load(ws, s, lane))); }
  wait_rt(T);
}
DEVI void gather_issue(v4u& d0, v4u& d1, rsrc_t ws, int s, int wave, const Gat& g) {
  asm volatile("" ::: "memory");
  d0 = ld16cs(ws, g.o0, W_MEM + s * 32768);
  d1 = ld16cs(ws, g.o1, W_MEM + s * 32768);
}
DEVI void gather_mem(v4u d0, v4u d1, const Args& a, rsrc_t ws, int s, unsigned char* lds, int wave, int lane, const Gat& g, unsigned T, int fk, unsigned long long& fc) {
  const int base = W_MEM + s * 32768;
  sts16(lds, g.l0, d0);
  sts16(lds, g.l1, d1);
  bool ok = umax(mx4(d0), mx4(d1)) != SENT;
  if (!__all(ok)) {
    FAILCNT(fk);
    unsigned spins = 0;
    for (;;) {
      if (spins == 1) FAILCNT(fk ? 7 : 1);
      T += D_MU;
      wait_rt(T);
      asm volatile("" ::: "memory");
      if (!ok) { d0 = ld16cs(ws, g.o0, base); if (wave >= 4) d1 = ld16cs(ws, g.o1, base); ok = umax(mx4(d0), mx4(d1)) != SENT; }
      if (__all(ok)) break;
      if (give_up(spins, ws)) break;
    }
    sts16(lds, g.l0, d0);
    if (wave >= 4) sts16(lds, g.l1, d1);
  }
}
DEVI void own_piece0(rsrc_t ws, unsigned char* lds, int un, int tid) {
  if (tid < 8) sts16(lds, mem_lds(un * 8 + tid), ld16c(ws, W_MEM + sprd((un * 8 + tid) * 16)));
}
template <int LO, int HI>
DEVI void s2_zload(bf8 (&z)[4], rsrc_t ws, int s, int un, int wave  , int zv) {
  const int zs = W_Z + s * (32 * 40960) + (4 * wave * 40 + (un >> 1)) * 1024;
#pragma unroll
  for (int i = LO; i < HI; ++i) z[i] = asbf8(ld16cs(ws, zv, zs + (i & 1) * 81920 + (i >> 1) * 512));
}
DEVI int t2_off(int e, int g) {
  const int kk = e >> 6, gm = (e >> 4) & 3, m = (e >> 2) & 3, half = (e >> 1) & 1, t = e & 1;
  return (kk * 32 + (m >> 1) * 16 + gm * 4 + (m & 1) * 2 + t) * 32 + half * 16 + g * 8;
}

#define S2_WORK(STB) \
      { \
        const int ou = u_base(s) + ((un >> 1) * 64 + 4 * wave) * 256, op = p_base(s) + ((un & 1) * 2 + (wave >> 2)) * 16384 + (wave & 3) * 1280; \
        T += x1(a, s); \
        wait_rt(T + D_U); \
        v4u uu = ld16cs(ws, l16, ou); \
        wait_rt(T + D_P); \
        PH(7); \
        v4u pt = ld16cs(ws, l16, op), pm = ld16cs(ws, pmv, op); \
        const f4 zz = {0, 0, 0, 0}; \
        f4 c1 = smfma(asbf8(pt), z[0], z[1], zz, idx); \
        f4 c2 = smfma(asbf8(pt), z[2], z[3], zz, idx); \
        c1 = mfma(asbf8(pm), asbf8(uu), c1); \
        nosched(); \
        bool kp = umax(mx4(pt), mx4(pm)) != SENT, ku = mx4(uu) != SENT; \
        if (!__all(kp & ku)) { \
          if (!__all(ku)) FAILCNT(5); \
          if (!__all(kp)) FAILCNT(4); \
          unsigned spins = 0, tq = T + D_P; \
          for (;;) { \
            if (s == 0) __builtin_amdgcn_s_sleep(5); else { tq += D_MU; wait_rt(tq); } \
            asm volatile("" ::: "memory"); \
            if (!ku) uu = ld16cs(ws, l16, ou); \
            if (!kp) { pt = ld16cs(ws, l16, op); pm = ld16cs(ws, pmv, op); } \
            kp = umax(mx4(pt), mx4(pm)) != SENT; ku = mx4(uu) != SENT; \
            if (__all(kp & ku)) break; \
            if (give_up(spins, ws)) break; \
          } \
          c1 = smfma(asbf8(pt), z[0], z[1], zz, idx); \
          c2 = smfma(asbf8(pt), z[2], z[3], zz, idx); \
          c1 = mfma(asbf8(pm), asbf8(uu), c1); \
        } \
        PH(8); \
        if (wave == 3) STAMP((STB) + 2); \
        if (g4 & 1) c1 = c2; \
        const float ra = swapadd32(c1[0], c1[2]), rb = swapadd32(c1[1], c1[3]); \
        const v2u sw = swap16(__builtin_bit_cast(unsigned, ra), __builtin_bit_cast(unsigned, rb)); \
        sts4(lds, L_RED + lane * 48 + wave * 4, __builtin_bit_cast(unsigned, u2f(sw.x) + u2f(sw.y))); \
        PH(9); \
      }
#define S2_FINAL(STB) \
      if (wave == 0) { \
        const f4 r4 = ldsf4(lds, L_RED + lane * 48) + ldsf4(lds, L_RED + lane * 48 + 16); \
        const float v = (r4[0] + r4[1]) + (r4[2] + r4[3]); \
        const unsigned dv = nosent(pk(v, dpp<0xB1>(v))); \
        st4cs(ws, (n & 1) ? OOB : g4 * 32 + (n & 2) * 8 + (n >> 2) * 4, W_MEM + (s + 1) * 32768 + sprd(un * 128), dv); \
        if (!(n & 1)) sts4(lds, mem_lds(un * 8 + g4 * 2 + ((n >> 1) & 1)) + (n >> 2) * 4, dv); \
        nosched(); \
        PH(11); \
        STAMP((STB) + 3); \
      } \
      T += x2(a, s, sh); \
      if (wave != 0) { \
        const unsigned tr = rt8(); \
        if (s == 0) T = (tr + D_Y) & ~31u; \
        while ((int)(tr - T) > D_Z0) { T += D_MU; if (wave == 1) FAILCNT(3); } \
      } \
      if (wave == 1) { \
        st4c(ws, lane == 0 && rep >= 0 ? W_TS + (s + 1) * 32768 + rep * 64 : OOB, T | 1u); \
      }

DEVI void role_A(const Args& a, int h, unsigned char* lds, int tid) {
  const int lane = tid & 63, wave = __builtin_amdgcn_readfirstlane(tid >> 6);
  const rsrc_t ws = mkrs(a.ws, WS_BYTES);
  bf8 w[2][20];
  load_w256(w, mkrs(wave < 4 ? a.wq : a.wk, 640 * 8192), ((tid & 255) >> 4) * 8192 + h * 256 + (tid & 15) * 16, 16 * 8192, (tid & 255) >> 4, (wave >> 2) * 16 + (tid & 15), lds, tid, [&](int k) { if (k == 0 && wave == 0) wait_prog(ws, C_XP, 1, 32u, lane, 1); });
  STAMP(3 * 64 * 64 * 4 + blockIdx.x * 4 + 3);
  const int idx = (lane & 8) ? 0xEEEE : 0x4444;
  const int n = lane & 15, g4 = lane >> 4;
  const int e = 32 * (wave & 3) + 8 * (n >> 2) + 4 * (lane >> 5) + (n & 3);
  const int wr_off = wave < 4 ? L_TQ + t2_off(e, g4 & 1) : L_TK + e * 16 + (g4 & 1) * 8;
  const int a4 = n >> 2, c = lane & 3;
  const int qrd = L_TQ + (g4 * 4 + a4) * 32 + c * 8;
  const int krd = L_TK + (8 * g4 + a4) * 16 + (c & 1) * 8;
  const int pbase = (wave * 2 + (h >> 4)) * 16384 + ((h >> 2) & 3) * 1280;
  const int pst = pbase + (h & 3) * 256 + (n & 1) * 128 + (n & 2) * 32 + g4 * 16 + a4 * 4;
  const int psm = (n & 9) ? OOB : pbase + 1024 + (h & 3) * 64 + g4 * 16 + (n >> 1) * 4;
  const int kxo = (8 * (c >> 1) + 2 * a4 + (c & 1) + 16 * g4) * 16;
  const Gat gt = gat_init(wave, lane, -1);
  const float cs = 0.08838834764831845f * 1.4426950408889634f;
  unsigned long long fc = 0;
  Sch sh = {0u, 0};
  PHDECL;
  auto run = [&](auto wc) {
    constexpr int W = decltype(wc)::v;
    unsigned tsv = 0u, T = rt8();
    bf8 kx[2][4];
    bool k0 = false;
    if (W < 2 && (D_Z & 1)) {
      k0 = __builtin_amdgcn_readfirstlane(ld4c(ws, W_CTL + C_KP + (h >> 1) * 4)) >= 1u;
      asm volatile("" ::: "memory");
#pragma unroll
      for (int i = 0; i < 8; ++i) kx[i >> 2][i & 3] = asbf8(ld16cs(ws, k0 ? kxo : OOB, W_KX + h * 8192 + i * 1024));
    }
    for (int s = 0; s < NSEG; ++s) {
      if (W == 2) {
        PH(0);
        v4u d0, d1;
        gather_issue(d0, d1, ws, s, wave, gt);
        gather_mem(d0, d1, a, ws, s, lds, wave, lane, gt, T, 0, fc);
      }
      PH(1);
      bar();
      PH(2);
      {
        bf8 af[5];
#pragma unroll
        for (int k = 0; k < 5; ++k) af[k] = asbf8(lds16(lds, L_M + k * 1024 + lane * 16));
        tsv = W == 2 ? ts_load(ws, s, lane) : ld4c(ws, W_CTL + C_KP + (h >> 1) * 4);
        nosched();
        f4 c0 = {0, 0, 0, 0}, c1 = {0, 0, 0, 0};
#pragma unroll
        for (int k = 0; k < 10; ++k) {
          c0 = smfma(af[k % 5], w[0][2 * k], w[0][2 * k + 1], c0, idx);
          c1 = smfma(af[k % 5], w[1][2 * k], w[1][2 * k + 1], c1, idx);
          if (k < 5) af[k % 5] = asbf8(lds16(lds, L_M + (k + 5) * 1024 + lane * 16));
          nosched();
        }
        v2u o; o.x = pk(swapadd32(c0[0], c1[0]), swapadd32(c0[1], c1[1])); o.y = pk(swapadd32(c0[2], c1[2]), swapadd32(c0[3], c1[3]));
        sts8(lds, wr_off, o);
      }
      PH(3);
      bar();
      PH(4);
      if (W < 2) {
        if (s == NSEG - 1) break;
        if (s == 0 && !k0) {
          wait_prog(ws, C_KP + (h >> 1) * 4, 1, 1u, lane, 1);
#pragma unroll
          for (int i = 0; i < 8; ++i) kx[i >> 2][i & 3] = asbf8(ld16cs(ws, kxo, W_KX + h * 8192 + i * 1024));
        }
        bf8 q0 = ldtr2(lds, qrd, qrd + 512), q1 = ldtr2(lds, qrd + 1024, qrd + 1536);
        bf8 km[4];
#pragma unroll
        for (int k = 0; k < 4; ++k) km[k] = ldtr2(lds, krd + k * 512, krd + k * 512 + 64);
        nosched();
        f4 d0 = {0, 0, 0, 0}, d1 = d0, d2 = d0;
        d1 = smfma(q0, kx[0][0], kx[0][1], d1, idx); d2 = smfma(q0, kx[1][0], kx[1][1], d2, idx);
        d1 = smfma(q1, kx[0][2], kx[0][3], d1, idx); d2 = smfma(q1, kx[1][2], kx[1][3], d2, idx);
        d0 = smfma(q0, km[0], km[1], d0, idx); d0 = smfma(q1, km[2], km[3], d0, idx);
        PH(5);
        const float s1 = swapadd32(d1[2 * W], d1[2 * W + 1]), s2 = swapadd32(d2[2 * W], d2[2 * W + 1]), s0 = swapadd32(d0[2 * W], d0[2 * W + 1]);
        const float mc = -cs * row16_max2(__builtin_fmaxf(__builtin_fmaxf(s1, s2), s0));
        float p1 = __builtin_amdgcn_exp2f(fmaf(s1, cs, mc)), p2 = __builtin_amdgcn_exp2f(fmaf(s2, cs, mc)), p0 = __builtin_amdgcn_exp2f(fmaf(s0, cs, mc));
        const float r = __builtin_amdgcn_rcpf(row16_sum(fmaf(p0, 0.5f, p1 + p2)));
        st4cs(ws, pst, p_base(s), nosent(pk(p1 * r, p2 * r)));
        p0 *= r;
        st4cs(ws, psm, p_base(s), nosent(pk(p0, dpp<0xB1>(p0))));
        nosched();
        PH(6);
        if (W == 1) STAMP(((0 * 64 + h) * 64 + s) * 4 + 2);
        if (__builtin_amdgcn_readfirstlane(tsv) < (unsigned)(s + 2)) { wait_prog(ws, C_KP + (h >> 1) * 4, 1, (unsigned)(s + 2), lane, 1); FAILCNT(7); }
#pragma unroll
        for (int i = 0; i < 8; ++i) kx[i >> 2][i & 3] = asbf8(ld16cs(ws, kxo, W_KX + ((s + 1) * 32 + h) * 8192 + i * 1024));
        PHC();
      } else {
        const unsigned tb2 = rt8();
        if (wave == 2) STAMP(((0 * 64 + h) * 64 + s) * 4 + 0);
        if (wave >= 4) {
          const int k = wave - 4;
          v4u kv = __builtin_bit_cast(v4u, ldtr2(lds, krd + k * 512, krd + k * 512 + 64));
          kv.x = nosent(kv.x); kv.y = nosent(kv.y); kv.z = nosent(kv.z); kv.w = nosent(kv.w);
          st16cs(ws, n < 8 ? g4 * 128 + n * 16 : OOB, W_KM + (s * 32 + h) * 2048 + k * 512, kv);
        }
        if (s == NSEG - 1) break;
        PHC();
        T = ts_ref(a, ws, s, lane, tsv, tb2 - D_R10, T, sh);
        T += x1(a, s) + x2(a, s, sh);
        gather_wait(a, ws, s + 1, lane, T);
      }
    }
  };
  if (wave == 0) run(IC<0>());
  else if (wave == 1) run(IC<1>());
  else run(IC<2>());
  FAILOUT(h);
  PHOUT(h);
}

DEVI void role_VO(const Args& a, int j, unsigned char* lds, int tid) {
  const int lane = tid & 63, wave = __builtin_amdgcn_readfirstlane(tid >> 6);
  const rsrc_t ws = mkrs(a.ws, WS_BYTES);
  const int h = j >> 1, un = j, rep = (un % 5) == 0 ? un / 5 : -1;
  const int tile0 = (j & 1) * 20 + (wave < 4 ? 3 * wave : 4 + 2 * wave);
  bf8 wv[20], wo[3][4];
  load_wvo<3>(wv, wo, a, h, wave < 7 ? tile0 : tile0 - 1, j & 1, lds, tid, [&](int k) { if (k == 0 && wave == 0) wait_prog(ws, C_XP, 1, 32u, lane, 1); });
  if (wave == 7) {
#pragma unroll
    for (int k = 0; k < 4; ++k) { wo[0][k] = wo[1][k]; wo[1][k] = wo[2][k]; }
  }
  STAMP(3 * 64 * 64 * 4 + blockIdx.x * 4 + 3);
  const int idx = (lane & 8) ? 0xEEEE : 0x4444;
  const int n = lane & 15, g4 = lane >> 4;
  const int e = 32 * (wave >> 1) + 8 * (n >> 2) + 4 * (wave & 1) + (n & 3);
  const int wr_off = L_TQ + t2_off(e, g4 & 1) + (lane >> 5) * 4;
  const int a4 = n >> 2, c = lane & 3;
  const int qrd = L_TQ + (g4 * 4 + a4) * 32 + c * 8;
  const int l16 = lane * 16, pmv = n < 4 ? 1024 + g4 * 64 + n * 16 : OOB;
  const Gat gt = gat_init(wave, lane, un);
  const int us_off = ((tile0 + (lane >> 5)) * 64 + h) * 256 + n * 16 + (g4 & 1) * 8, us2_off = ((tile0 + 2) * 64 + h) * 256 + n * 16 + (g4 & 1) * 8 + (lane >> 5) * 4;
  bf8 z[4];
  unsigned tsv, zq, T = rt8();
  const int zqo = wave == 1 ? W_CTL + C_ZP + (lane & 31) * 4 : OOB;
  own_piece0(ws, lds, un, tid);
  int zv = OOB;
  unsigned long long fc = 0;
  Sch sh = {0u, 0};
  PHDECL;
  for (int s = 0; s < NSEG; ++s) {
    PH(0);
    {
      v4u d0, d1;
      if (wave >= 2) gather_issue(d0, d1, ws, s, wave, gt);
      nosched();
      if (wave >= 2) gather_mem(d0, d1, a, ws, s, lds, wave, lane, gt, T, 2, fc);
    }
    PH(1);
    bar();
    PH(2);
    if (wave == 2) STAMP(((2 * 64 + j) * 64 + s) * 4 + 0);
    {
      bf8 af[10];
#pragma unroll
      for (int k = 0; k < 10; ++k) af[k] = asbf8(lds16(lds, L_M + k * 1024 + lane * 16));
      nosched();
      tsv = ts_load(ws, s, lane);
      zq = ld4cs(ws, zqo, (~s & 1) * 128);
      s2_zload<0, 1>(z, ws, s, un, wave, zv);
      nosched();
      f4 c0 = {0, 0, 0, 0};
#pragma unroll
      for (int k = 0; k < 10; ++k) {
        c0 = smfma(af[k], wv[2 * k], wv[2 * k + 1], c0, idx);
        if (k == 3) s2_zload<1, 2>(z, ws, s, un, wave, zv);
        if (k == 7) s2_zload<2, 3>(z, ws, s, un, wave, zv);
        nosched();
      }
      sts4(lds, wr_off, pk(swapadd32(c0[0], c0[2]), swapadd32(c0[1], c0[3])));
      PH(3);
      bar();
      PH(4);
      bf8 v0 = ldtr2(lds, qrd, qrd + 512), v1 = ldtr2(lds, qrd + 1024, qrd + 1536);
      nosched();
      s2_zload<3, 4>(z, ws, s, un, wave, zv);
      nosched();
      const f4 zz = {0, 0, 0, 0};
      f4 u0 = smfma(v0, wo[0][0], wo[0][1], zz, idx), u1 = smfma(v0, wo[1][0], wo[1][1], zz, idx);
      u0 = smfma(v1, wo[0][2], wo[0][3], u0, idx); u1 = smfma(v1, wo[1][2], wo[1][3], u1, idx);
      f4 u2 = zz;
      if (wave < 4) { u2 = smfma(v0, wo[2][0], wo[2][1], zz, idx); u2 = smfma(v1, wo[2][2], wo[2][3], u2, idx); }
      v2u o = {nosent(pk(swapadd32(u0[0], u1[0]), swapadd32(u0[1], u1[1]))), nosent(pk(swapadd32(u0[2], u1[2]), swapadd32(u0[3], u1[3])))};
      st8cs(ws, us_off, u_base(s), o);
      if (wave < 4) st4cs(ws, us2_off, u_base(s), nosent(pk(swapadd32(u2[0], u2[2]), swapadd32(u2[1], u2[3]))));
      PH(5);
      if (wave == 2) STAMP(((2 * 64 + j) * 64 + s) * 4 + 1);
    }
    if (s == NSEG - 1) break;
    if (s == 0) {
      if (wave == 1) wait_prog(ws, C_ZP, 32, 1u, lane, (D_Z >> 2) & 7);
      bar();
      zv = (lane >> 5) * 40960 + (lane & 31) * 16;
      s2_zload<0, 4>(z, ws, s, un, wave, zv);
    }
    T = ts_ref(a, ws, s, lane, tsv, rt8() - D_Y0, T, sh);
    S2_WORK(((2 * 64 + j) * 64 + s) * 4)
    bar();
    PH(10);
    S2_FINAL(((2 * 64 + j) * 64 + s) * 4)
    if (wave == 1 && !__all(zq >= (unsigned)(s + 2))) { wait_prog(ws, C_ZP + (~s & 1) * 128, 32, (unsigned)(s + 2), lane, 1); FAILCNT(6); }
    PHC();
    if (wave >= 2) gather_wait(a, ws, s + 1, lane, T);
  }
  FAILOUT(32 + j);
  PHOUT(32 + j);
}

DEVI void role_S2(const Args& a, int un, unsigned char* lds, int tid) {
  const int lane = tid & 63, wave = __builtin_amdgcn_readfirstlane(tid >> 6);
  const rsrc_t ws = mkrs(a.ws, WS_BYTES);
  const int rep = (un % 5) == 0 ? un / 5 : -1;
  const int idx = (lane & 8) ? 0xEEEE : 0x4444;
  const int n = lane & 15, g4 = lane >> 4;
  const int l16 = lane * 16, pmv = n < 4 ? 1024 + g4 * 64 + n * 16 : OOB;
  const Gat gt = gat_init(wave, lane, un);
  const int zv = (lane >> 5) * 40960 + (lane & 31) * 16;
  bf8 z[4];
  unsigned T = 0;
  unsigned zq = 0u;
  const int zqo = wave == 1 ? W_CTL + C_ZP + (lane & 31) * 4 : OOB;
  unsigned long long fc = 0;
  Sch sh = {0u, 0};
  PHDECL;
  for (int s = 0; s < NSEG - 1; ++s) {
    if (wave == 1 && !__all(zq >= (unsigned)(s + 1))) wait_prog(ws, C_ZP + (s & 1) * 128, 32, (unsigned)(s + 1), lane, (D_Z >> 2) & 7);
    if (s > 0 && wave >= 2) { gather_wait(a, ws, s, lane, T); v4u d0, d1; gather_issue(d0, d1, ws, s, wave, gt); gather_mem(d0, d1, a, ws, s, lds, wave, lane, gt, T, 2, fc); }
    bar();
    const unsigned tb1 = rt8();
    const unsigned tsv = ts_load(ws, s, lane);
    zq = ld4cs(ws, zqo, (~s & 1) * 128);
    s2_zload<0, 4>(z, ws, s, un, wave, zv);
    PH(5);
    T = ts_ref(a, ws, s, lane, tsv, tb1 - D_TS, T, sh);
    S2_WORK(((1 * 64 + un - 64) * 64 + s) * 4)
    bar();
    PH(10);
    S2_FINAL(((1 * 64 + un - 64) * 64 + s) * 4)
    PHC();
  }
  FAILOUT(32 + un);
  PHOUT(32 + un);
}

constexpr int L_X0 = 0, L_X1 = 40960, L_AUX = 81920;
DEVI void xn_fetch(v4u (&st)[5], rsrc_t ws, int s, int tid) {
#pragma unroll
  for (int j = 0; j < 5; ++j) st[j] = ld16c(ws, s < NSEG ? W_XN + s * 40960 + (j * 512 + tid) * 16 : OOB);
}
DEVI void xn_put(const v4u (&st)[5], unsigned char* lds, int base, int tid) {
#pragma unroll
  for (int j = 0; j < 5; ++j) sts16(lds, base + (j * 512 + tid) * 16, st[j]);
}
DEVI void gemm256(f4 (&acc)[2][2], const bf8 (&w)[2][20], unsigned char* lds, int xb, int xo, rsrc_t ws, int sn, int tid) {
  const int lane = tid & 63;
  const int vo = sn < NSEG ? tid * 16 : OOB, so = W_XN + sn * 40960;
#pragma unroll
  for (int u = 0; u < 2; ++u) { acc[u][0] = (f4){0, 0, 0, 0}; acc[u][1] = (f4){0, 0, 0, 0}; }
  bf8 x[3][2];
  v4u st[5];
#pragma unroll
  for (int k = 0; k < 2; ++k) { x[k][0] = asbf8(lds16(lds, xb + k * 1024 + lane * 16)); x[k][1] = asbf8(lds16(lds, xb + 20480 + k * 1024 + lane * 16)); }
#pragma unroll
  for (int k = 0; k < 20; ++k) {
    if (k < 18) { x[(k + 2) % 3][0] = asbf8(lds16(lds, xb + (k + 2) * 1024 + lane * 16)); x[(k + 2) % 3][1] = asbf8(lds16(lds, xb + 20480 + (k + 2) * 1024 + lane * 16)); }
    if (k >= 1 && k < 6) st[k - 1] = ld16cs(ws, vo, so + (k - 1) * 8192);
    if (k >= 14 && k < 19) sts16(lds, xo + ((k - 14) * 512 + tid) * 16, st[k - 14]);
    nosched();
    acc[0][0] = mfma(w[0][k], x[k % 3][0], acc[0][0]); acc[1][0] = mfma(w[1][k], x[k % 3][0], acc[1][0]);
    acc[0][1] = mfma(w[0][k], x[k % 3][1], acc[0][1]); acc[1][1] = mfma(w[1][k], x[k % 3][1], acc[1][1]);
    nosched();
  }
}
DEVI v4u pack8(f4 a, f4 b) { v4u o = {pk(a[0], a[1]), pk(a[2], a[3]), pk(b[0], b[1]), pk(b[2], b[3])}; return o; }

template <int MODE>
DEVI void role_W256(const Args& a, int sl, int par, unsigned char* lds, int tid) {
  const int lane = tid & 63, wave = __builtin_amdgcn_readfirstlane(tid >> 6);
  const rsrc_t ws = mkrs(a.ws, WS_BYTES);
  v4u st[5];
  constexpr int STP = MODE == 0 ? 1 : 2;
  bf8 w[2][20];
  auto mid = [&](int k) { if (k == 0 && wave == 0) wait_prog(ws, C_XP, 1, 32u, lane, 1); if (k == 1) xn_fetch(st, ws, par, tid); if (wave == 7 && k == 1) STAMP(3 * 64 * 64 * 4 + 2048 + blockIdx.x); };
  if (MODE != 1) load_w256(w, mkrs(MODE ? a.wq : a.wk, 640 * 8192), (tid >> 5) * 8192 + sl * 512 + (tid & 31) * 16, 16 * 8192, tid >> 5, tid & 31, lds, tid, mid);
  else load_w256(w, mkrs(a.w1, 640 * 3456), sl * 256 + (tid & 31) * 8 < 1728 ? (tid >> 5) * 3456 + sl * 512 + (tid & 31) * 16 : OOB, 16 * 3456, tid >> 5, tid & 31, lds, tid, mid);
  STAMP(3 * 64 * 64 * 4 + blockIdx.x * 4 + 3);
  const int n = lane & 15, g4 = lane >> 4;
  v4u bias = {0, 0, 0, 0};
  if (MODE == 1) bias = ld16(mkrs(a.b1, 3456), (sl * 256 + wave * 32 + g4 * 8) * 2);
  xn_put(st, lds, L_X0, tid);
  bar();
  const v4u sent = {SENT, SENT, SENT, SENT};
  unsigned kprog = 0;
  int it = 0;
  PHWDECL;
  for (int s = par; s < NSEG; s += STP, ++it) {
    const int xb = (it & 1) ? L_X1 : L_X0, xo = (it & 1) ? L_X0 : L_X1;
    PHW(0);
    if (it == 3) wait_prog(ws, C_XP + 128, 1, 48u, lane, 1);
    f4 acc[2][2];
    gemm256(acc, w, lds, xb, xo, ws, s + STP, tid);
    PHW(2);
    if (MODE == 0) {
      const int hd = 2 * sl + (wave >> 2);
#pragma unroll
      for (int rt = 0; rt < 2; ++rt) st16c(ws, W_KX + ((s * 32 + hd) * 8 + rt * 4 + (wave & 3)) * 1024 + lane * 16, pack8(acc[0][rt], acc[1][rt]));
      if (wave < 4) st16c(ws, s ? W_KM + (s * 32 + 2 * sl) * 2048 + tid * 16 : OOB, sent);
    }
    if (MODE == 1) {
      const int kk = sl * 8 + wave;
#pragma unroll
      for (int rt = 0; rt < 2; ++rt) {
        v4u pv = pack8(acc[0][rt], acc[1][rt]), o;
#pragma unroll
        for (int j = 0; j < 4; ++j) {
          unsigned hb = pk(blo(pv[j]) + blo(bias[j]), bhi(pv[j]) + bhi(bias[j]));
          float lo = blo(hb), hi = bhi(hb);
          lo = lo > 0.f ? lo : 0.f; hi = hi > 0.f ? hi : 0.f;
          o[j] = pk(lo * lo, hi * hi);
        }
        st16c(ws, kk < 54 ? W_H + ((s * 2 + rt) * 54 + kk) * 1024 + lane * 16 : OOB, o);
      }
    }
    if (MODE == 2) {
      const int ab = L_AUX + (it & 1) * 16384;
#pragma unroll
      for (int rt = 0; rt < 2; ++rt) sts16(lds, ab + (((wave >> 2) * 2 + rt) * 4 + (wave & 3)) * 1024 + lane * 16, pack8(acc[0][rt], acc[1][rt]));
      drain();
      bar();
      if (tid == 256 && it > 0) st4c(ws, W_CTL + C_QP + par * 64 + sl * 4, (unsigned)(s + 1 - STP));
      if (wave < 4) {
        const int hh = wave >> 1, rt = wave & 1, hd = 2 * sl + hh;
        if (kprog < (unsigned)(s + 1)) kprog = wait_prog(ws, C_KP + sl * 4, 1, (unsigned)(s + 1), lane, 2);
        bf8 kf[2][4], qf[4];
        v4u km[4]; unsigned spins = 0;
        const int ko = n < 8 ? W_KM + (s * 32 + hd) * 2048 + g4 * 128 + n * 16 : OOB;
#pragma unroll
        for (int i = 0; i < 8; ++i) kf[i >> 2][i & 3] = asbf8(ld16c(ws, W_KX + (s * 32 + hd) * 8192 + i * 1024 + lane * 16));
#pragma unroll
        for (int k = 0; k < 4; ++k) km[k] = ld16c(ws, n < 8 ? ko + k * 512 : OOB);
#pragma unroll
        for (int k = 0; k < 4; ++k) qf[k] = asbf8(lds16(lds, ab + ((hh * 2 + rt) * 4 + k) * 1024 + lane * 16));
        nosched();
        f4 d0 = {0, 0, 0, 0}, d1 = d0, d2 = d0;
#pragma unroll
        for (int k = 0; k < 4; ++k) { d1 = mfma(kf[0][k], qf[k], d1); d2 = mfma(kf[1][k], qf[k], d2); }
        nosched();
        for (;;) {
          if ((D_R0 & 1) || __all(valid4(km[0]) & valid4(km[1]) & valid4(km[2]) & valid4(km[3]))) break;
          if (give_up(spins, ws)) break;
          __builtin_amdgcn_s_sleep(4);
          asm volatile("" ::: "memory");
#pragma unroll
          for (int k = 0; k < 4; ++k) km[k] = ld16c(ws, n < 8 ? ko + k * 512 : OOB);
        }
#pragma unroll
        for (int k = 0; k < 4; ++k) d0 = mfma(asbf8(km[k]), qf[k], d0);
        const float sc = 0.08838834764831845f, NEG = -1.0e9f;
        const int q = 16 * rt + n;
        float v[12];
#pragma unroll
        for (int i = 0; i < 4; ++i) {
          v[i] = g4 < 2 ? rbf(rbf(d0[i]) * sc) : -INFINITY;
          v[4 + i] = (4 * g4 + i <= q) ? rbf(rbf(d1[i]) * sc) : NEG;
          v[8 + i] = (16 + 4 * g4 + i <= q) ? rbf(rbf(d2[i]) * sc) : NEG;
        }
        float m = v[0];
#pragma unroll
        for (int i = 1; i < 12; ++i) m = fmaxf(m, v[i]);
        m = x4_max(m);
        float l = 0.f;
#pragma unroll
        for (int i = 0; i < 12; ++i) { v[i] = __builtin_amdgcn_exp2f((v[i] - m) * 1.4426950408889634f); l += v[i]; }
        l = x4_sum(l);
        const float r = 1.0f / l;
        v4u pt = {nosent(pk(v[4] * r, v[8] * r)), nosent(pk(v[5] * r, v[9] * r)), nosent(pk(v[6] * r, v[10] * r)), nosent(pk(v[7] * r, v[11] * r))};
        if (s == 1) {
          st16c(ws, W_PT + (NSEG - 1) * 81920 + (hd * 2 + rt) * 1024 + lane * 16, sent);
          st16c(ws, g4 == 0 ? W_PT + (NSEG - 1) * 81920 + 65536 + ((hd >> 2) * 2 + rt) * 1024 + ((hd & 3) * 16 + n) * 16 : OOB, sent);
        }
        st16c(ws, W_PT + s * 81920 + (hd * 2 + rt) * 1024 + lane * 16, pt);
        const unsigned mx = nosent(pk(v[0] * r, v[1] * r)), my = nosent(pk(v[2] * r, v[3] * r));
        const v2u sx = swap16(mx, mx), sy = swap16(my, my);
        v4u pm = {sx.x, sy.x, sx.y, sy.y};
        st16c(ws, g4 == 0 ? W_PT + s * 81920 + 65536 + ((hd >> 2) * 2 + rt) * 1024 + ((hd & 3) * 16 + n) * 16 : OOB, pm);
      }
      if (s + STP < NSEG) continue;
    }
    PHW(3);
    if (it == 0 && wave == 0) STAMP(3 * 64 * 64 * 4 + 1024 + blockIdx.x * 4 + 1);
    const bool full = MODE == 2 || it < 2 || s + STP >= NSEG;
    if (full) drain(); else asm volatile("s_waitcnt vmcnt(2)" ::: "memory");
    PHW(5);
    bar();
    if (it < 2 && wave == 0) STAMP(3 * 64 * 64 * 4 + 1024 + blockIdx.x * 4 + 2 + it);
    PHW(6);
    if (tid == 0) st4c(ws, W_CTL + (MODE == 0 ? C_KP : MODE == 1 ? C_HP + par * 32 : C_QP + par * 64) + sl * 4, (unsigned)(full ? s + 1 : s + 1 - STP));
  }
  PHWOUT(112 + blockIdx.x);
}

DEVI void role_VZ(const Args& a, int h, int par, unsigned char* lds, int tid) {
  const int lane = tid & 63, wave = __builtin_amdgcn_readfirstlane(tid >> 6);
  const rsrc_t ws = mkrs(a.ws, WS_BYTES);
  v4u st[5];
  bf8 wv[20], wo[5][4];
  load_wvo<5>(wv, wo, a, h, 5 * wave, par ^ ((D_Z >> 1) & 1), lds, tid, [&](int k) { if (k == 0 && wave == 0) wait_prog(ws, C_XP, 1, 32u, lane, 1); if (k == 1) xn_fetch(st, ws, par, tid); if (wave == 7 && k == 1) STAMP(3 * 64 * 64 * 4 + 2048 + blockIdx.x); });
  STAMP(3 * 64 * 64 * 4 + blockIdx.x * 4 + 3);
  xn_put(st, lds, L_X0, tid);
  bar();
  const int n = lane & 15, g4 = lane >> 4;
  const v4u sent = {SENT, SENT, SENT, SENT};
  int it = 0;
  PHWDECL;
  const int send = par ? D_S0 : NSEG;
  int sp = 0;
  for (int s = par; s < send; ++it) {
    const int xb = (it & 1) ? L_X1 : L_X0, xo = (it & 1) ? L_X0 : L_X1;
    const int sn = s + (s < D_S0 ? 2 : 1);
    PHW(0);
    if (it == 3) wait_prog(ws, C_XP + 128, 1, 48u, lane, 1);
    xn_fetch(st, ws, sn < send ? sn : NSEG, tid);
    PHW(1);
    f4 v0 = {0, 0, 0, 0}, v1 = v0;
#pragma unroll
    for (int k = 0; k < 20; ++k) {
      bf8 x0 = asbf8(lds16(lds, xb + k * 1024 + lane * 16)), x1 = asbf8(lds16(lds, xb + 20480 + k * 1024 + lane * 16));
      v0 = mfma(wv[k], x0, v0); v1 = mfma(wv[k], x1, v1);
    }
    {
      v2u o0 = {pk(v0[0], v0[1]), pk(v0[2], v0[3])}, o1 = {pk(v1[0], v1[1]), pk(v1[2], v1[3])};
      const int o = L_AUX + (wave >> 1) * 1024 + lane * 16 + (wave & 1) * 8;
      sts8(lds, o, o0); sts8(lds, o + 4096, o1);
    }
    PHW(2);
    st16c(ws, s ? u_base(s) + ((tid >> 4) * 64 + h) * 256 + (tid & 15) * 16 : OOB, sent);
    st16c(ws, s && tid < 128 ? u_base(s) + ((32 + (tid >> 4)) * 64 + h) * 256 + (tid & 15) * 16 : OOB, sent);
    PHW(7);
    bar();
    PHW(8);
    if (it == 0 && wave == 0) STAMP(3 * 64 * 64 * 4 + 1024 + blockIdx.x * 4 + 0);
    bf8 vf[2][4];
#pragma unroll
    for (int i = 0; i < 8; ++i) vf[i >> 2][i & 3] = asbf8(lds16(lds, L_AUX + i * 1024 + lane * 16));
#pragma unroll
    for (int t = 0; t < 5; ++t) {
      f4 z0 = {0, 0, 0, 0}, z1 = z0;
#pragma unroll
      for (int k = 0; k < 4; ++k) { z0 = mfma(vf[0][k], wo[t][k], z0); z1 = mfma(vf[1][k], wo[t][k], z1); }
      v4u o = {pk(z0[0], z1[0]), pk(z0[1], z1[1]), pk(z0[2], z1[2]), pk(z0[3], z1[3])};
      st16c(ws, W_Z + ((s * 32 + h) * 40 + 5 * wave + t) * 1024 + lane * 16, o);
    }
    PHW(3);
    xn_put(st, lds, xo, tid);
    PHW(4);
    if (it == 0 && wave == 0) STAMP(3 * 64 * 64 * 4 + 1024 + blockIdx.x * 4 + 1);
    const bool full = it < 2 || sn >= send;
    if (full) drain(); else asm volatile("s_waitcnt vmcnt(7)" ::: "memory");
    PHW(5);
    bar();
    if (it < 2 && wave == 0) STAMP(3 * 64 * 64 * 4 + 1024 + blockIdx.x * 4 + 2 + it);
    PHW(6);
    if (wave == 0) {
      if (s == D_S0 + 1) wait_prog(ws, C_ZP + 128 + h * 4, 1, (unsigned)D_S0, lane, 2);
      if (lane == 0) {
        if (it > 0 && (!full || ((sp ^ s) & 1))) st4c(ws, W_CTL + C_ZP + (sp & 1) * 128 + h * 4, (unsigned)(sp + 1));
        if (full) st4c(ws, W_CTL + C_ZP + (s & 1) * 128 + h * 4, (unsigned)(s + 1));
      }
    }
    sp = s; s = sn;
  }
  PHWOUT(112 + blockIdx.x);
}

DEVI void role_F2(const Args& a, int cg, int par, unsigned char* lds, int tid) {
  const int lane = tid & 63, wave = __builtin_amdgcn_readfirstlane(tid >> 6);
  const rsrc_t ws = mkrs(a.ws, WS_BYTES);
  const int n = lane & 15, g4 = lane >> 4;
  bf8 w[5][7];
  {
    const rsrc_t r2 = mkrs(a.w2, 1728 * 1280);
    constexpr int RS = 192;
    v4u st[2][5];
    int go[5], lo[5];
#pragma unroll
    for (int j = 0; j < 5; ++j) { const int u = j * 64 + lane, r = u / 10, uc = u - r * 10; go[j] = (224 * wave + r) * 1280 + cg * 160 + uc * 16; lo[j] = r * RS + uc * 16; }
#pragma unroll
    for (int j = 0; j < 5; ++j) st[0][j] = __builtin_amdgcn_raw_buffer_load_b128(r2, go[j], 0, 0);
    const int a4 = n >> 2, c = lane & 3;
    const int wb = wave * (2 * 32 * RS);
#pragma unroll
    for (int k = 0; k < 7; ++k) {
      const int base = wb + (k & 1) * 32 * RS;
#pragma unroll
      for (int j = 0; j < 5; ++j) sts16(lds, base + lo[j], st[k & 1][j]);
      if (k < 6) {
#pragma unroll
        for (int j = 0; j < 5; ++j) st[~k & 1][j] = __builtin_amdgcn_raw_buffer_load_b128(r2, go[j] + (k + 1) * 32 * 1280, 0, 0);
      }
      asm volatile("s_waitcnt lgkmcnt(0)" ::: "memory");
#pragma unroll
      for (int t = 0; t < 5; ++t) { const int o = base + (8 * g4 + a4) * RS + t * 32 + c * 8; w[t][k] = ldtr2(lds, o, o + 4 * RS); }
      asm volatile("" ::: "memory");
    }
    bar();
  }
  if (tid < 10) sts16(lds, 81920 + tid * 16, ld16(mkrs(a.b2, 1280), cg * 160 + tid * 16));
  unsigned hprog = wait_prog(ws, C_HP + (par & 1) * 32, 7, (unsigned)(par + 1), lane, 2);
  const int nk = wave == 7 ? 5 : 7;
  const int hv = lane * 16;
  bf8 hf[2][7];
#pragma unroll
  for (int k = 0; k < 7; ++k)
#pragma unroll
    for (int rt = 0; rt < 2; ++rt) hf[rt][k] = asbf8(ld16cs(ws, hv, W_H + ((par * 2 + rt) * 54 + 7 * wave + (k < nk ? k : 0)) * 1024));
  for (int s = par; s < NSEG; s += D_NC) {
    const int sn = s + D_NC < NSEG ? s + D_NC : s;
    if (wave == 0 && hprog < (unsigned)(sn + 1)) hprog = wait_prog(ws, C_HP + (par & 1) * 32, 7, (unsigned)(sn + 1), lane, 2);
    drain();
    bar();
    if (tid == 0 && s >= D_NC) st4c(ws, W_CTL + C_GP + par * 64 + cg * 4, (unsigned)(s + 1 - D_NC));
#pragma unroll
    for (int rt = 0; rt < 2; ++rt) {
      f4 acc[5];
#pragma unroll
      for (int t = 0; t < 5; ++t) acc[t] = (f4){0, 0, 0, 0};
#pragma unroll
      for (int k = 0; k < 7; ++k) {
        if (k == 5 && wave == 7) break;
#pragma unroll
        for (int t = 0; t < 5; ++t) acc[t] = mfma(w[t][k], hf[rt][k], acc[t]);
        hf[rt][k] = asbf8(ld16cs(ws, hv, W_H + ((sn * 2 + rt) * 54 + 7 * wave + k) * 1024));
        nosched();
      }
#pragma unroll
      for (int t = 0; t < 5; ++t) stsf4(lds, (wave * 10 + 2 * t + rt) * 1024 + lane * 16, acc[t]);
    }
    bar();
#pragma unroll 1
    for (int rep = 0; rep < 2; ++rep) {
      if (rep == 1 && wave >= 2) break;
      nosched();
      const int item = rep * 8 + wave;
      f4 r = ldsf4(lds, item * 1024 + lane * 16);
#pragma unroll
      for (int i = 1; i < 8; ++i) r += ldsf4(lds, (i * 10 + item) * 1024 + lane * 16);
      const v2u bv = *(const LAS v2u*)(LAS const unsigned char*)(lds + 81920 + (item >> 1) * 32 + g4 * 8);
      float o[4];
      const float bb[4] = {blo(bv.x), bhi(bv.x), blo(bv.y), bhi(bv.y)};
#pragma unroll
      for (int i = 0; i < 4; ++i) {
        const float y = rbf(rbf(r[i]) + bb[i]);
        o[i] = y / (1.0f + __expf(-y));
      }
      const unsigned gx = pk(o[0], o[1]), gy = pk(o[2], o[3]);
      const v2u sx = swap16(gx, gx), sy = swap16(gy, gy);
      v4u ov = {sx.x, sy.x, sx.y, sy.y};
      st16c(ws, !(g4 & 1) ? W_G + ((s * 8 + cg) * 10 + item) * 512 + ((g4 >> 1) * 16 + n) * 16 : OOB, ov);
    }
  }
  drain();
  bar();
  if (tid == 0) st4c(ws, W_CTL + C_GP + par * 64 + cg * 4, (unsigned)NSEG);
}

DEVI void role_out(const Args& a, bool spare, unsigned char* lds, int tid) {
  const int lane = tid & 63, wave = __builtin_amdgcn_readfirstlane(tid >> 6);
  const rsrc_t ws = mkrs(a.ws, WS_BYTES);
  const rsrc_t rx = mkrs(a.x, 2048 * 1280), ro = mkrs(a.out, 2048 * 1280);
  const int n = lane & 15, g4 = lane >> 4;
  auto grab = [&] { return __hip_atomic_fetch_add((GAS unsigned*)(a.ws + W_CTL + C_TASK), 1u, __ATOMIC_RELAXED, __HIP_MEMORY_SCOPE_AGENT); };
  auto slot = [&](int i) { return __builtin_amdgcn_readfirstlane((int)*(const LAS unsigned*)(LAS unsigned char*)(lds + 131072 + i * 4)); };
  const int pz = lane < 32 ? 128 : lane < 48 ? 64 : 0, pgm = lane >= 48 ? 64 : 0;
  const int pb = tid < 32 ? C_ZP + lane * 4 : tid < 48 ? C_QP + (lane - 32) * 4 : tid < 56 ? C_GP + (lane - 48) * 4 : OOB;
  auto prog = [&](int t) { const int s = t / 10; return ld4c(ws, W_CTL + pb + (s & 1) * pz + (s & (D_NC - 1)) * pgm); };
  int task = 0;
  unsigned pv = 0u;
  bool pre = false;
  for (int it = 0;; ++it) {
    if (!pre) {
      if (tid == 0) sts4(lds, 131072 + (2 + (it & 1)) * 4, grab());
      bar();
      task = slot(2 + (it & 1));
      pv = prog(task);
    }
    if (task >= NSEG * 10) break;
    pre = task < NSEG * 10 - 80 && !spare;
    unsigned tk = 0u;
    if (tid == 0 && pre) tk = grab();
    const int s = task / 10, cg = task - s * 10, par = s & 1, rb = (it & 1) * 65536;
    const bool all = wave != 0 || __all(pv >= (unsigned)(s + 1) || lane >= 56);
    const int ga = D_GA + (spare && task < D_G3 ? D_G2 - D_GA : task < D_G1 || task >= D_X ? D_G0 - D_GA : 0);
    if (!all) wait_far(ws, C_ZP + par * 128, 32, (unsigned)(s + 1), 2u, lane);
    bar();
    bf8 y[5][4];
#pragma unroll
    for (int hh = 0; hh < 4; ++hh)
#pragma unroll
      for (int t = 0; t < 4; ++t) {
        y[hh][t] = asbf8(ld16c(ws, W_Z + ((s * 32 + 4 * wave + hh) * 40 + cg * 4 + t) * 1024 + lane * 16));
        for (int i = 0; i < ga; ++i) __builtin_amdgcn_s_sleep(1);
      }
    const int tok = s * 32 + (wave >> 2) * 16 + n;
    const int xo = tok * 1280 + (cg * 64 + (wave & 3) * 16 + g4 * 4) * 2;
    const v2u xv = ld8(rx, xo);
    if (!all) {
      wait_far(ws, C_GP + (s & (D_NC - 1)) * 64, 8, (unsigned)(s + 1), (unsigned)D_NC, lane);
      wait_far(ws, C_QP + par * 64, 16, (unsigned)(s < NSEG - 1 ? s + 1 : s - 1), 2u, lane);
      if (s == NSEG - 1) wait_rt(ts_get(ws, s, lane, 0u) + D_A0);
    }
    bar();
    const v2u gv = ld8c(ws, W_G + ((s * 40 + cg * 4 + (wave & 3)) * 2 + (wave >> 2)) * 512 + ((g4 >> 1) * 16 + n) * 16 + (g4 & 1) * 8);
    v4u p[5][2], uu[4];
    unsigned spins = 0;
    for (;;) {
      asm volatile("" ::: "memory");
#pragma unroll
      for (int hh = 0; hh < 4; ++hh) {
#pragma unroll
        for (int rt = 0; rt < 2; ++rt) p[hh][rt] = ld16c(ws, W_PT + s * 81920 + ((4 * wave + hh) * 2 + rt) * 1024 + lane * 16);
        for (int i = 0; i < (s < 62 ? ga : 0); ++i) __builtin_amdgcn_s_sleep(1);
      }
#pragma unroll
      for (int rt = 0; rt < 2; ++rt) p[4][rt] = ld16c(ws, W_PT + s * 81920 + 65536 + (wave * 2 + rt) * 1024 + lane * 16);
#pragma unroll
      for (int t = 0; t < 4; ++t) uu[t] = ld16c(ws, u_base(s) + ((cg * 4 + t) * 64 + 4 * wave) * 256 + lane * 16);
      unsigned m = umax(umax(mx4(uu[0]), mx4(uu[1])), umax(mx4(uu[2]), mx4(uu[3])));
#pragma unroll
      for (int i = 0; i < 10; ++i) m = umax(m, mx4(p[i >> 1][i & 1]));
      if (__all(m != SENT)) break;
      if (give_up(spins, ws)) break;
    }
#pragma unroll
    for (int t = 0; t < 4; ++t) y[4][t] = asbf8(uu[t]);
    f4 acc[2][4];
#pragma unroll
    for (int i = 0; i < 8; ++i) acc[i >> 2][i & 3] = (f4){0, 0, 0, 0};
#pragma unroll
    for (int hh = 0; hh < 5; ++hh)
#pragma unroll
      for (int rt = 0; rt < 2; ++rt)
#pragma unroll
        for (int t = 0; t < 4; ++t) acc[rt][t] = mfma(y[hh][t], asbf8(p[hh][rt]), acc[rt][t]);
#pragma unroll
    for (int i = 0; i < 8; ++i) stsf4(lds, rb + (wave * 8 + i) * 1024 + lane * 16, acc[i >> 2][i & 3]);
    if (tid == 0) sts4(lds, 131072 + (it & 1) * 4, tk);
    bar();
    if (pre) { task = slot(it & 1); pv = prog(task); }
    f4 r = ldsf4(lds, rb + wave * 1024 + lane * 16);
#pragma unroll
    for (int i = 1; i < 8; ++i) r += ldsf4(lds, rb + (i * 8 + wave) * 1024 + lane * 16);
    const float gg[4] = {blo(gv.x), bhi(gv.x), blo(gv.y), bhi(gv.y)}, xx[4] = {blo(xv.x), bhi(xv.x), blo(xv.y), bhi(xv.y)};
    float o[4];
#pragma unroll
    for (int i = 0; i < 4; ++i) o[i] = xx[i] + rbf(rbf(r[i]) * gg[i]);
    v2u ov = {pk(o[0], o[1]), pk(o[2], o[3])};
    st8(ro, xo, ov);
    if (s >= NSEG - 2 || (spare && task >= D_G3)) break;
  }
}

template <int N>
DEVI void touch(const uint16_t* p, int off, int step, int bytes = 640 * 8192) {
  const rsrc_t r = mkrs(p, bytes);
  unsigned v[N];
#pragma unroll
  for (int j = 0; j < N; ++j) v[j] = __builtin_amdgcn_raw_buffer_load_b32(r, off + j * step, 0, 0);
#pragma unroll
  for (int j = 0; j < N; ++j) asm volatile("" ::"v"(v[j]));
}
DEVI void role_ln(const Args& a, int s, int half, int hp, unsigned char* lds, int tid) {
  const int lane = tid & 63, wave = __builtin_amdgcn_readfirstlane(tid >> 6);
  const rsrc_t ws = mkrs(a.ws, WS_BYTES);
  const int n = lane & 15, hf = half < 0 ? wave >> 2 : half, tn = (wave & 3) * 4 + (lane >> 4), t8 = tid & 255;
  const bool doln = half < 0 || wave < 4;
  v4u d[5];
  if (doln) {
    const rsrc_t rx = mkrs(a.x, 2048 * 1280);
#pragma unroll
    for (int j = 0; j < 5; ++j) d[j] = ld16(rx, (s * 32 + hf * 16 + tn) * 1280 + (n + 16 * j) * 16);
  }
  if (half < 0 || wave >= 4) {
    const v4u sent = {SENT, SENT, SENT, SENT}, zero = {0, 0, 0, 0};
    if (half >= 0) {
#pragma unroll
      for (int j = 0; j < 3; ++j) st16c(ws, j * 256 + t8 < 640 ? W_U + ((j * 16 + (t8 >> 4)) * 64 + hp) * 256 + (t8 & 15) * 16 : W_KM + hp * 2048 + (t8 - 128) * 16, sent);
    }
    if (hf) {
#pragma unroll
      for (int j = 0; j < 5; ++j) st16c(ws, p_base(s) + (t8 >> 6) * 16384 + (j * 64 + (t8 & 63)) * 16, sent);
    } else {
      st16c(ws, t8 < 16 ? W_TS + s * 32768 + t8 * 64 : OOB, zero);
      if (s > 0) {
#pragma unroll
        for (int j = 0; j < 3; ++j) st16c(ws, j * 256 + t8 < 640 ? W_MEM + s * 32768 + sprd((j * 256 + t8) * 16) : OOB, sent);
      } else {
        const rsrc_t rm = mkrs(a.mem0, 8 * 1280);
        unsigned v[10];
#pragma unroll
        for (int j = 0; j < 10; ++j) v[j] = __builtin_amdgcn_raw_buffer_load_b32(rm, (j * 256 + t8) * 4, 0, 0);
#pragma unroll
        for (int j = 0; j < 10; ++j) {
          const int i = j * 256 + t8, r = i / 320, pp = i - r * 320, t = pp >> 3, p = pp & 7;
          st4c(ws, W_MEM + sprd(t * 256) + ((r >> 1) & 1) * 128 + ((r & 1) * 2 + (r >> 2)) * 32 + (p & 1) * 16 + (p >> 1) * 4, nosent(v[j]));
        }
      }
    }
  }
  if (doln) {
    float sum = 0.f;
#pragma unroll
    for (int j = 0; j < 20; ++j) sum += blo(d[j >> 2][j & 3]) + bhi(d[j >> 2][j & 3]);
    const float mean = row16_sum(sum) * (1.0f / 640.0f);
    float sq = 0.f;
#pragma unroll
    for (int j = 0; j < 20; ++j) { const float t = blo(d[j >> 2][j & 3]) - mean, u = bhi(d[j >> 2][j & 3]) - mean; sq = fmaf(t, t, fmaf(u, u, sq)); }
    sq = row16_sum(sq);
    const float mb = rbf(mean), vb = rbf(sq * (1.0f / 640.0f));
    const float sd = rbf(sqrtf(rbf(vb + 1e-05f))), rc = 1.0f / sd;
#pragma unroll
    for (int j = 0; j < 5; ++j) {
      v4u o;
#pragma unroll
      for (int i = 0; i < 4; ++i) {
        const unsigned c = pk(blo(d[j][i]) - mb, bhi(d[j][i]) - mb);
        const float x0 = blo(c), x1 = bhi(c), q0 = x0 * rc, q1 = x1 * rc;
        o[i] = pk(fmaf(fmaf(-q0, sd, x0), rc, q0), fmaf(fmaf(-q1, sd, x1), rc, q1));
      }
      sts16(lds, hf * 20480 + (n + 16 * j) * 256 + ((tn + n) & 15) * 16, o);
    }
  }
  bar();
  if (doln) {
#pragma unroll
    for (int j = 0; j < 5; ++j) {
      const int u = j * 256 + t8;
      st16c(ws, W_XN + s * 40960 + hf * 20480 + u * 16, lds16(lds, hf * 20480 + (u >> 4) * 256 + ((u + (u >> 4)) & 15) * 16));
    }
  }
  drain();
  bar();
  if (tid == 0) __hip_atomic_fetch_add((GAS unsigned*)(a.ws + W_CTL + C_XP + (s < 16 ? 0 : 128)), 1u, __ATOMIC_RELAXED, __HIP_MEMORY_SCOPE_AGENT);
}

__global__ void __launch_bounds__(512, 2) rmt_main(Args a) {
  extern __shared__ __attribute__((aligned(16))) unsigned char lds[];
  const int tid = threadIdx.x;
  a.ws += -(uintptr_t)a.ws & (uintptr_t)(2 * MB - 1);
  int j0 = 0;
  {
    unsigned m[8], best = ~0u;
#pragma unroll
    for (int j = 0; j < 8; ++j) m[j] = ((const GAS unsigned*)(a.ws + W_CAL))[j] + ((const GAS unsigned*)(a.ws + W_CAL))[j + 8];
#pragma unroll
    for (int j = 0; j < 8; ++j) { const unsigned w = m[j] + m[(j + 1) & 7] + m[(j + 2) & 7] + m[(j + 3) & 7]; if (w < best) { best = w; j0 = j; } }
  }
  const int xc = (blockIdx.x - j0) & 7, ix = blockIdx.x >> 3, y = xc - 4, b = ix * 8 + xc;
  if (!(D_R0 & 8)) {
    if (xc >= 4 && ix >= 20 && ix < 28) touch<2>(a.wk, (639 - (ix & 1) * 224 - (tid >> 2)) * 8192 + (4 * y + ((ix - 20) >> 1)) * 512 + (tid & 3) * 128, -96 * 8192);
  }
  if (ix >= (xc < 4 ? 24 : 20)) role_ln(a, xc < 4 ? 4 * xc + (ix & 3) : 16 + 12 * y + ix - 20, xc < 4 ? (ix >> 2) & 1 : -1, 8 * xc + ix - 24, lds, tid);
  if (xc < 4) {
    if (ix < 8) role_A(a, 8 * xc + ix, lds, tid);
    else if (ix < 24) role_VO(a, 16 * xc + ix - 8, lds, tid);
    else if (ix < 28) role_S2(a, 64 + 4 * xc + ix - 24, lds, tid);
    if (ix < 28 || D_G2 == 0) return;
    if (D_A1 & 3) {
      wait_rt(rt8() + D_A1);
      if (D_A1 & 1) touch<3>(a.w1, ((4 * xc + ix - 28) * 512 + tid) * 128, MB, 640 * 3456);
      if (D_A1 & 2) touch<3>(a.w2, ((4 * xc + ix - 28) * 512 + tid) * 128, MB, 640 * 3456);
    }
    if (tid < 64) wait_far(mkrs(a.ws, WS_BYTES), C_GP, 8, 1u, 0u, tid);
  }
  else {
  if (ix >= 20) wait_rt(rt8() + D_Q1);
  if (ix < 16) {
    role_VZ(a, 8 * y + (ix >> 1), ix & 1, lds, tid);
    if ((ix & 1) && (D_NC == 4 || ix < 8)) { bar(); role_F2(a, D_NC == 4 ? ix >> 1 : (y >> 1) * 4 + (ix >> 1), y & (D_NC - 1), lds, tid); }
  }
  else if (ix < 20) role_W256<0>(a, 4 * y + ix - 16, 0, lds, tid);
  else if (ix < 28) role_W256<2>(a, 4 * y + ((ix - 20) >> 1), ix & 1, lds, tid);
  else if (4 * y + ix - 28 < 14) role_W256<1>(a, (4 * y + ix - 28) >> 1, ix & 1, lds, tid);
  }
  bar();
  if (!(D_R0 & 3)) role_out(a, xc < 4, lds, tid);
}

__global__ void __launch_bounds__(512) rmt_pre(Args a) {
  __shared__ unsigned lat[16];
  const int tid = threadIdx.x, wave = tid >> 6, b = blockIdx.x;
  a.ws += -(uintptr_t)a.ws & (uintptr_t)(2 * MB - 1);
  const rsrc_t ws = mkrs(a.ws, WS_BYTES);
  const v4u zero = {0, 0, 0, 0};
  st16(ws, b == 0 && tid < 128 ? W_CTL + tid * 16 : OOB, zero);
  const int o = (tid & 63) ? OOB : 4 * MB + (b * 8 + wave) * 16384 + ((b + wave) & 7) * 256;
  st4c(ws, o + 8192, 0u);
  unsigned b0 = 0u, b1 = 0u;
#pragma unroll 1
  for (int k = 0; k < 3; ++k) {
    asm volatile("s_waitcnt vmcnt(0)\n\ts_barrier" ::: "memory");
    const unsigned t0 = now();
    asm volatile("s_waitcnt lgkmcnt(0)" ::: "memory");
    st4c(ws, o + (k & 1) * 8192, 0u);
    asm volatile("s_waitcnt vmcnt(0)" ::: "memory");
    b1 = b0; b0 = now() - t0;
  }
  lat[wave * 2] = b0; lat[wave * 2 + 1] = b1;
  __syncthreads();
  if (tid < 64) {
    const bool slow = tid < 8 && lat[2 * tid] > lat[2 * tid + 1];
    unsigned c = __builtin_popcountll(__ballot(slow));
    if (tid == 0) *(GAS unsigned*)(a.ws + W_CAL + b * 4) = c;
  }
}

extern "C" void kernel_launch(void* const* d_in, const int* in_sizes, int n_in, void* d_out, int out_size, void* d_ws, size_t ws_size,
                              hipStream_t stream) {
  Args a;
  a.x = (const uint16_t*)d_in[0]; a.mem0 = (const uint16_t*)d_in[1]; a.wq = (const uint16_t*)d_in[2]; a.wk = (const uint16_t*)d_in[3];
  a.wv = (const uint16_t*)d_in[4]; a.wo = (const uint16_t*)d_in[5]; a.w1 = (const uint16_t*)d_in[8]; a.b1 = (const uint16_t*)d_in[9];
  a.w2 = (const uint16_t*)d_in[10]; a.b2 = (const uint16_t*)d_in[11]; a.out = (uint16_t*)d_out; a.ws = (unsigned char*)d_ws;
  constexpr int LDS_BYTES = 148 * 1024;
  (void)hipFuncSetAttribute((const void*)rmt_main, hipFuncAttributeMaxDynamicSharedMemorySize, LDS_BYTES);
  rmt_pre<<<16, 512, 0, stream>>>(a);
  rmt_main<<<256, 512, LDS_BYTES, stream>>>(a);
}
```
